```python
import torch

def _softmax(x, axis=-1):
    return torch.softmax(x, dim=axis)
def _gelu(x):
    return torch.nn.functional.gelu(x, approximate="tanh")
def _silu(x):
    return torch.nn.functional.silu(x)
def _sigmoid(x):
    return torch.sigmoid(x)
def _layernorm(x, axis=-1, eps=1e-5):
    m = x.mean(dim=axis, keepdim=True)
    v = x.var(dim=axis, keepdim=True, unbiased=False)
    return (x - m) / torch.sqrt(v + eps)
def _scatter_add(tgt, idx, src, axis=0):
    return tgt.clone().index_add_(axis, idx, src)
def _segment_sum(src, seg, n):
    out = torch.zeros((n,) + src.shape[1:], dtype=src.dtype)
    return out.index_add_(0, seg, src)
def _pad(x, pw):
    flat = tuple(v for pair in reversed(pw) for v in pair)
    return torch.nn.functional.pad(x, flat)

kernel_name = 'sequential_preLN__mha_block_cached__dense_activation_gelu__decode'

def setup_inputs():

    _w = (lambda a: (lambda t: t.to(torch.bfloat16) if t.is_floating_point() else t)(torch.as_tensor(a)))
    def _A_setup():
        B, Sq, D = 2, 1, 512
        H, Sk, Dh = 32, 4096, 128
        return {
            "x": _w((lambda *s: torch.randn(*s, dtype=torch.bfloat16) * 0.1)(B, Sq, D)),
            "k_cache": _w((lambda *s: torch.randn(*s, dtype=torch.bfloat16) * 1.6)(B, H, Sk, Dh)),
            "v_cache": _w((lambda *s: torch.randn(*s, dtype=torch.bfloat16) * 1.6)(B, H, Sk, Dh)),
            "wq": _w((lambda *s: torch.randn(*s, dtype=torch.bfloat16) * 1.6)(D, H * Dh) * (D ** -0.5)),
            "wo": _w((lambda *s: torch.randn(*s, dtype=torch.bfloat16) * 1.6)(H * Dh, D) * ((H * Dh) ** -0.5)),
        }
    _A_in = _A_setup()

    def _B_setup():
        B, S, D, F = 2, 1, 512, 1376
        return {
            "x": _w((lambda *s: torch.randn(*s, dtype=torch.bfloat16) * 0.1)(B, S, D)),
            "w1": _w((lambda *s: torch.randn(*s, dtype=torch.bfloat16) * 3.2)(D, F) * (D ** -0.5)),
            "b1": _w((lambda *s: torch.randn(*s, dtype=torch.bfloat16) * 3.2)(F) * 0.01),
            "w2": _w((lambda *s: torch.randn(*s, dtype=torch.bfloat16) * 3.2)(F, D) * (F ** -0.5)),
            "b2": _w((lambda *s: torch.randn(*s, dtype=torch.bfloat16) * 3.2)(D) * 0.01),
        }
    _B_in = _B_setup()

    return {
        "A_x": _A_in["x"],
        "A_k_cache": _A_in["k_cache"],
        "A_v_cache": _A_in["v_cache"],
        "A_wq": _A_in["wq"],
        "A_wo": _A_in["wo"],
        "B_x": _B_in["x"],
        "B_w1": _B_in["w1"],
        "B_b1": _B_in["b1"],
        "B_w2": _B_in["w2"],
        "B_b2": _B_in["b2"],
    }

def reference(A_x, A_k_cache, A_v_cache, A_wq, A_wo, B_x, B_w1, B_b1, B_w2, B_b2):
    def _A_ref(x, k_cache, v_cache, wq, wo):
        B, Sq, D = x.shape
        H, Dh = 32, 128
        q = (lambda x, *d: x.permute(*d))((x @ wq).reshape(B, Sq, H, Dh), 0, 2, 1, 3)
        scores = torch.einsum("bhqd,bhkd->bhqk", q, k_cache) * (Dh ** -0.5)
        p = _softmax(scores, axis=-1)
        o = torch.einsum("bhqk,bhkd->bhqd", p, v_cache)
        return (lambda x, *d: x.permute(*d))(o, 0, 2, 1, 3).reshape(B, Sq, H * Dh) @ wo

    def _B_ref(x, w1, b1, w2, b2):
        h = torch.einsum("bsd,df->bsf", x, w1) + b1
        h = _gelu(h)
        return torch.einsum("bsf,fd->bsd", h, w2) + b2

    x = A_x
    h = x + _A_ref(_layernorm(x, axis=-1), A_k_cache, A_v_cache, A_wq, A_wo)
    h2 = h + _B_ref(_layernorm(h, axis=-1), B_w1, B_b1, B_w2, B_b2)
    return h2
```

```cpp
#include <hip/hip_runtime.h>
#include <hip/hip_bf16.h>
#include <stdint.h>

typedef unsigned v4u __attribute__((ext_vector_type(4)));
typedef unsigned short u16;
constexpr int B = 2, D = 512, H = 32, SK = 4096, DH = 128, F = 1376;
#define MIMIC 1

__device__ __forceinline__ float bf2f(u16 b) { return __uint_as_float((unsigned)b << 16); }
__device__ __forceinline__ float rbf(float f) { return __bfloat162float(__float2bfloat16(f)); }
__device__ __forceinline__ float mr(float f) { return MIMIC ? rbf(f) : f; }

__device__ float block_sum(float v, float* red) {
  for (int o = 32; o > 0; o >>= 1) v += __shfl_xor(v, o);
  int w = threadIdx.x >> 6;
  __syncthreads();
  if ((threadIdx.x & 63) == 0) red[w] = v;
  __syncthreads();
  float s = 0;
  for (int i = 0; i < (int)(blockDim.x >> 6); ++i) s += red[i];
  return s;
}
__device__ float block_max(float v, float* red) {
  for (int o = 32; o > 0; o >>= 1) v = fmaxf(v, __shfl_xor(v, o));
  int w = threadIdx.x >> 6;
  __syncthreads();
  if ((threadIdx.x & 63) == 0) red[w] = v;
  __syncthreads();
  float s = -INFINITY;
  for (int i = 0; i < (int)(blockDim.x >> 6); ++i) s = fmaxf(s, red[i]);
  return s;
}

__device__ void ln_row(float* row, float* red) {
  float a = row[threadIdx.x], b = row[threadIdx.x + 256];
  float mean = block_sum(a + b, red) * (1.f / D);
  float var = block_sum((a - mean) * (a - mean) + (b - mean) * (b - mean), red) * (1.f / D);
  float m = mr(mean), v = mr(var);
  float den = mr(sqrtf(mr(v + 1e-5f)));
  __syncthreads();
  row[threadIdx.x] = mr(mr(a - m) / den);
  row[threadIdx.x + 256] = mr(mr(b - m) / den);
  __syncthreads();
}

__global__ void k_q(const u16* x, const u16* wq, float* q) {
  __shared__ float row[D]; __shared__ float red[8];
  int b = blockIdx.y;
  row[threadIdx.x] = bf2f(x[b * D + threadIdx.x]);
  row[threadIdx.x + 256] = bf2f(x[b * D + threadIdx.x + 256]);
  __syncthreads();
  ln_row(row, red);
  int n = blockIdx.x * 256 + threadIdx.x;
  float acc = 0;
  for (int d = 0; d < D; ++d) acc += row[d] * bf2f(wq[(size_t)d * (H * DH) + n]);
  q[b * H * DH + n] = mr(acc);
}

__global__ void k_attn(const float* q, const u16* kc, const u16* vc, float* o) {
  __shared__ float s[SK]; __shared__ float qs[DH]; __shared__ float red[8]; __shared__ float part[2][DH];
  int bh = blockIdx.x;
  if (threadIdx.x < DH) qs[threadIdx.x] = q[bh * DH + threadIdx.x];
  __syncthreads();
  const u16* K = kc + (size_t)bh * SK * DH; const u16* V = vc + (size_t)bh * SK * DH;
  float mx = -INFINITY;
  for (int k = threadIdx.x; k < SK; k += 256) {
    float a = 0;
    for (int d = 0; d < DH; ++d) a += qs[d] * bf2f(K[(size_t)k * DH + d]);
    a = mr(mr(a) * 0.08838834764831845f);
    s[k] = a; mx = fmaxf(mx, a);
  }
  mx = block_max(mx, red);
  float sum = 0;
  for (int k = threadIdx.x; k < SK; k += 256) { float e = expf(s[k] - mx); s[k] = e; sum += e; }
  sum = block_sum(sum, red);
  __syncthreads();
  int d = threadIdx.x & 127, half = threadIdx.x >> 7;
  float acc = 0;
  for (int k = half; k < SK; k += 2) acc += mr(s[k] / sum) * bf2f(V[(size_t)k * DH + d]);
  part[half][d] = acc;
  __syncthreads();
  if (threadIdx.x < DH) o[bh * DH + threadIdx.x] = mr(part[0][threadIdx.x] + part[1][threadIdx.x]);
}

__global__ void k_wo(const u16* x, const float* o, const u16* wo, float* h) {
  int b = blockIdx.y, n = blockIdx.x * 256 + threadIdx.x;
  float acc = 0;
  for (int k = 0; k < H * DH; ++k) acc += o[b * H * DH + k] * bf2f(wo[(size_t)k * D + n]);
  h[b * D + n] = mr(bf2f(x[b * D + n]) + mr(acc));
}
__global__ void k_m1(const float* h, const u16* w1, const u16* b1, float* g) {
  __shared__ float row[D]; __shared__ float red[8];
  int b = blockIdx.y;
  row[threadIdx.x] = h[b * D + threadIdx.x];
  row[threadIdx.x + 256] = h[b * D + threadIdx.x + 256];
  __syncthreads();
  ln_row(row, red);
  int n = blockIdx.x * 256 + threadIdx.x;
  if (n >= F) return;
  float acc = 0;
  for (int d = 0; d < D; ++d) acc += row[d] * bf2f(w1[(size_t)d * F + n]);
  float z = mr(mr(acc) + bf2f(b1[n]));
  float t = tanhf(0.7978845608028654f * (z + 0.044715f * z * z * z));
  g[b * F + n] = mr(0.5f * z * (1.f + t));
}
__global__ void k_m2(const float* h, const float* g, const u16* w2, const u16* b2, __hip_bfloat16* out) {
  int b = blockIdx.y, n = blockIdx.x * 256 + threadIdx.x;
  float acc = 0;
  for (int k = 0; k < F; ++k) acc += g[b * F + k] * bf2f(w2[(size_t)k * D + n]);
  out[b * D + n] = __float2bfloat16(h[b * D + n] + mr(mr(acc) + bf2f(b2[n])));
}

extern "C" void kernel_launch(void* const* d_in, const int* in_sizes, int n_in,
                              void* d_out, int out_size, void* d_ws, size_t ws_size,
                              hipStream_t stream) {
  const u16* x = (const u16*)d_in[0]; const u16* kc = (const u16*)d_in[1]; const u16* vc = (const u16*)d_in[2];
  const u16* wq = (const u16*)d_in[3]; const u16* wo = (const u16*)d_in[4];
  const u16* w1 = (const u16*)d_in[6]; const u16* b1 = (const u16*)d_in[7];
  const u16* w2 = (const u16*)d_in[8]; const u16* b2 = (const u16*)d_in[9];
  float* ws = (float*)d_ws;
  float* q = ws; float* o = ws + 8192; float* h = ws + 16384; float* g = ws + 20480;
  k_q<<<dim3(16, 2), 256, 0, stream>>>(x, wq, q);
  k_attn<<<64, 256, 0, stream>>>(q, kc, vc, o);
  k_wo<<<dim3(2, 2), 256, 0, stream>>>(x, o, wo, h);
  k_m1<<<dim3(6, 2), 256, 0, stream>>>(h, w1, b1, g);
  k_m2<<<dim3(2, 2), 256, 0, stream>>>(h, g, w2, b2, (__hip_bfloat16*)d_out);
}
```

```cpp
#include <hip/hip_runtime.h>
#include <stdint.h>

typedef unsigned short u16;
typedef unsigned v4u __attribute__((ext_vector_type(4)));
typedef unsigned v2u __attribute__((ext_vector_type(2)));
typedef short v4s __attribute__((ext_vector_type(4)));
typedef __bf16 bf16x2 __attribute__((ext_vector_type(2)));
typedef __bf16 bf16x8 __attribute__((ext_vector_type(8)));
typedef float f32x4 __attribute__((ext_vector_type(4)));
typedef float f32x2 __attribute__((ext_vector_type(2)));
typedef __amdgpu_buffer_rsrc_t rsrc_t;
#define GAS __attribute__((address_space(1)))
#define LAS __attribute__((address_space(3)))

#define NQ0 3
#define NQ2 30
#define DIST 6
#define ENDV 5
#define TAU4 140
#define LEAD 2
#define W_AUX 2
#define DRY 1
#define REPA 8
#define D1 0
#define D2 12
#define D3 0
#define CPF 1
#define PFLY 4
#define RT1 2
#define FWAIT 800
#define QWAIT 550

constexpr int NB = 2, DM = 512, NH = 32, SK = 4096, DH = 128, FF = 1376;
constexpr int NWG = 256, NTHR = 576;
constexpr int OFF_EPOCH = 0;
constexpr int OFF_FLAG = 1024;
constexpr int OFF_LSE = 4096;
constexpr int OFF_OP = 65536;
constexpr int OFF_QX = 131072;
constexpr int OFF_PO = OFF_QX + NH * 2 * 8 * 1024;
constexpr int PO_CG = 43 * 128;
constexpr int OFF_AP = OFF_PO + 32 * PO_CG;
constexpr int WS_BYTES = OFF_AP + 8 * 16384;
constexpr int OFF_STAMP = 1 << 20;
constexpr int AUX_SC1 = 16;
constexpr int OOB = 0x7ffffff0;
constexpr unsigned SENT = 0xFFFFFFFFu;
constexpr unsigned SPIN_CAP = 1u << 16;
constexpr int M1_BASE = 0, M1_N = 43;
constexpr int M2_BASE = 64, M2_N = 32;
constexpr int L_Q = 0;
constexpr int L_OP = 512;
constexpr int L_P1 = 5120;
constexpr int L_P2 = 7424;
constexpr int L_ST = 12032;
constexpr int L_WO = 12288;
constexpr int L_WQ = 35328;
constexpr int L_GX = 52736;
constexpr int L_TR = 54272;
constexpr int TR_W = 9216, TR_W2 = 4608;
constexpr int LDS_BYTES = L_TR + 8 * TR_W;

template <int CTRL> __device__ __forceinline__ float dpp(float x) {
  return __builtin_bit_cast(float, __builtin_amdgcn_mov_dpp(__builtin_bit_cast(int, x), CTRL, 0xf, 0xf, true));
}
constexpr int XOR1 = 0xB1, XOR2 = 0x4E, XOR7 = 0x141, ROR1 = 0x121, ROR2 = 0x122, ROR4 = 0x124, ROR8 = 0x128, BCAST = 0x150;
__device__ __forceinline__ float lo16(unsigned v) { return __uint_as_float(v << 16); }
__device__ __forceinline__ float hi16(unsigned v) { return __uint_as_float(v & 0xffff0000u); }
__device__ __forceinline__ unsigned pk2(float a, float b) {
  f32x2 p = {a, b};
  return __builtin_bit_cast(unsigned, __builtin_convertvector(p, bf16x2));
}
__device__ __forceinline__ float rbf(float a) { return lo16(pk2(a, a)); }
__device__ __forceinline__ float dot2(unsigned a, unsigned b, float c) {
  return __builtin_amdgcn_fdot2_f32_bf16(__builtin_bit_cast(bf16x2, a), __builtin_bit_cast(bf16x2, b), c, false);
}
__device__ __forceinline__ f32x4 mfma(v4u a, v4u b, f32x4 c) {
  return __builtin_amdgcn_mfma_f32_16x16x32_bf16(__builtin_bit_cast(bf16x8, a), __builtin_bit_cast(bf16x8, b), c, 0, 0, 0);
}
__device__ __forceinline__ float tr32(float a, float b) {
  auto s = __builtin_amdgcn_permlane32_swap(__float_as_uint(a), __float_as_uint(b), false, false);
  return __uint_as_float(s[0]) + __uint_as_float(s[1]);
}
__device__ __forceinline__ float tr16(float a, float b) {
  auto s = __builtin_amdgcn_permlane16_swap(__float_as_uint(a), __float_as_uint(b), false, false);
  return __uint_as_float(s[0]) + __uint_as_float(s[1]);
}
__device__ __forceinline__ float wave_sum(float x) {
  x += dpp<ROR8>(x); x += dpp<ROR4>(x); x += dpp<ROR2>(x); x += dpp<ROR1>(x);
  x = tr16(x, x); x = tr32(x, x);
  return x;
}
__device__ __forceinline__ float wave_max(float x) {
  x = fmaxf(x, dpp<ROR8>(x)); x = fmaxf(x, dpp<ROR4>(x)); x = fmaxf(x, dpp<ROR2>(x)); x = fmaxf(x, dpp<ROR1>(x));
  auto s = __builtin_amdgcn_permlane16_swap(__float_as_uint(x), __float_as_uint(x), false, false);
  x = fmaxf(__uint_as_float(s[0]), __uint_as_float(s[1]));
  auto t = __builtin_amdgcn_permlane32_swap(__float_as_uint(x), __float_as_uint(x), false, false);
  return fmaxf(__uint_as_float(t[0]), __uint_as_float(t[1]));
}
template <int CTRL> __device__ __forceinline__ float trstep(float lo, float hi, bool bit) {
  const float keep = bit ? hi : lo, send = bit ? lo : hi;
  return keep + dpp<CTRL>(send);
}
__device__ __forceinline__ rsrc_t mkrs(const void* p, int bytes) {
  return __builtin_amdgcn_make_buffer_rsrc((void*)p, 0, bytes, 0x00020000);
}
template <int AUX> __device__ __forceinline__ v4u ld128(rsrc_t rs, int voff, int soff) {
  return __builtin_amdgcn_raw_buffer_load_b128(rs, voff, soff, AUX);
}
__device__ __forceinline__ void lds_barrier() {
  asm volatile("s_waitcnt lgkmcnt(0)" ::: "memory");
  __builtin_amdgcn_s_barrier();
  asm volatile("" ::: "memory");
}
__device__ __forceinline__ void lds_barrier_if(int on) {
  asm volatile("s_waitcnt lgkmcnt(0)\n\ts_cmp_eq_u32 %0, 0\n\ts_cbranch_scc1 1f\n\ts_barrier\n1:" :: "s"(on) : "scc", "memory");
}
__device__ __forceinline__ bool has_sent(v4u v) { return (v.x == SENT) | (v.y == SENT) | (v.z == SENT) | (v.w == SENT); }
__device__ __forceinline__ unsigned nosent(unsigned v) { return v < SENT ? v : SENT - 1u; }
constexpr int brev4(int i) { return ((i & 1) << 3) | ((i & 2) << 1) | ((i & 4) >> 1) | ((i & 8) >> 3); }
__device__ __forceinline__ void pace(unsigned target) {
  while ((int)((unsigned)__builtin_amdgcn_s_memrealtime() - target) < 0) __builtin_amdgcn_s_sleep(1);
}
__device__ __forceinline__ v4u trfrag(LAS unsigned char* p, int rs4) {
  const v2u u0 = __builtin_bit_cast(v2u, __builtin_amdgcn_ds_read_tr16_b64_v4i16((LAS v4s*)p));
  const v2u u1 = __builtin_bit_cast(v2u, __builtin_amdgcn_ds_read_tr16_b64_v4i16((LAS v4s*)(p + rs4)));
  return v4u{u0.x, u0.y, u1.x, u1.y};
}
__device__ __forceinline__ void repoll128(v4u& a, rsrc_t rs, int off) {
  _Pragma("nounroll") for (unsigned spins = 0; spins < SPIN_CAP; ++spins) {
    const bool bad = has_sent(a);
    if (__builtin_amdgcn_ballot_w64(bad) == 0ull) break;
    asm volatile("" : "+v"(off) :: "memory");
    if (bad) a = ld128<AUX_SC1>(rs, off, 0);
  }
}
__device__ __forceinline__ float half_sum(float x) {
  x += dpp<ROR8>(x); x += dpp<ROR4>(x); x += dpp<ROR2>(x); x += dpp<ROR1>(x);
  return tr16(x, x);
}

template <int I> __device__ __forceinline__ void axpy8(float (&acc)[8], float pv, v4u v) {
  const float p = dpp<BCAST + I>(pv);
#pragma unroll
  for (int k = 0; k < 4; ++k) {
    acc[2 * k] = fmaf(p, lo16(v[k]), acc[2 * k]);
    acc[2 * k + 1] = fmaf(p, hi16(v[k]), acc[2 * k + 1]);
  }
}
__device__ __forceinline__ void pin8(float (&a)[8]) {
  asm volatile("" : "+v"(a[0]), "+v"(a[1]), "+v"(a[2]), "+v"(a[3]), "+v"(a[4]), "+v"(a[5]), "+v"(a[6]), "+v"(a[7]));
}
__device__ __forceinline__ void pin1(float& a) { asm volatile("" : "+v"(a)); }
constexpr int kissued(int i) { return (NQ2 + (i + 3) / 4 > i + DIST) ? NQ2 + (i + 3) / 4 : i + DIST; }
static_assert(kissued(32) == 32 + DIST, "the distance is reached inside the K phase");
template <int I, int N, typename F> __device__ __forceinline__ void sfor(F&& f) {
  if constexpr (I < N) { f(std::integral_constant<int, I>{}); sfor<I + 1, N>(f); }
}

#define STAMP(i) do {} while (0)

extern "C" __global__ void __launch_bounds__(NTHR)
fused_block(const u16* __restrict__ x, const u16* __restrict__ kc, const u16* __restrict__ vc,
            const u16* __restrict__ wq, const u16* __restrict__ wo, const u16* __restrict__ w1,
            const u16* __restrict__ b1, const u16* __restrict__ w2, const u16* __restrict__ b2,
            u16* out, unsigned char* ws) {
  __shared__ __attribute__((aligned(16))) unsigned char smem[LDS_BYTES];
  LAS unsigned char* L = (LAS unsigned char*)smem;
  const unsigned t0 = (unsigned)__builtin_amdgcn_s_memrealtime();
  const int tid = threadIdx.x, lane = tid & 63, w = (__builtin_amdgcn_readfirstlane(tid >> 6) + 8) % 9;
  const int r = lane >> 4, c = lane & 15;
  const int bid = blockIdx.x;
  const int kr = bid & 7, cg = bid >> 3;
  const int h = kr * 4 + (cg & 3), b = bid >> 7, s = (bid >> 5) & 3;
  const int bh = b * NH + h;
  const int l16 = lane * 16;
  const rsrc_t rs = mkrs(ws, WS_BYTES);
  const bool is_m1 = cg < 6 && kr * 6 + cg < M1_N;
  const bool is_m2 = bid >= M2_BASE && bid < M2_BASE + M2_N;
  const int mrole = is_m1 ? kr * 6 + cg : bid - M2_BASE;
  constexpr float LOG2E = 1.4426950408889634f;
  const f32x4 z4 = {0.f, 0.f, 0.f, 0.f};
  const v4u zero4 = {0u, 0u, 0u, 0u};
  const v4u ones = {0x3F803F80u, 0x3F803F80u, 0x3F803F80u, 0x3F803F80u};
  v4u ve[ENDV], gl = zero4, wov[2] = {zero4, zero4};
#pragma unroll
  for (int i = 0; i < ENDV; ++i) ve[i] = zero4;
  float acc[8], pr1 = 0.f, mw = 0.f, biasf = 0.f;
#pragma unroll
  for (int k = 0; k < 8; ++k) acc[k] = 0.f;
  unsigned tagA, tagB, xo = 0u;
  const int lse_off = OFF_LSE + (bh * 32 + (lane & 31)) * 16;
  const int ap_rep = (c & 3) + 4 * (r >> 1);
  const int ap_st = ap_rep < REPA ? OFF_AP + ap_rep * 16384 + ((cg >> 1) * 16 + kr * 2 + (r & 1)) * 64 + (cg & 1) * 32 + (c >> 2) * 8 : OOB;

  if (w < 8) {
    v4u kv[64];
    const size_t kvoff = ((size_t)bh * SK + s * 1024 + w * 128) * DH;
    const rsrc_t rk = mkrs(kc + kvoff, 32768), rv = mkrs(vc + kvoff, 32768);
    auto issue = [&](auto N) {
      constexpr int n = N.value;
      if constexpr (n < 64) {
        if constexpr (TAU4 > 0 && n >= NQ0 && ((n - NQ0) & 1) == 0) pace(t0 + (unsigned)((n - LEAD) * TAU4 / 4));
        if constexpr (n >= PFLY && n < kissued(32 - DIST)) asm volatile("s_waitcnt vmcnt(%0)" :: "i"(PFLY - 1));
        if constexpr (n < 32) kv[n] = ld128<2>(rk, l16, n * 1024); else kv[n] = ld128<2>(rv, l16, (n - 32) * 1024);
        __builtin_amdgcn_sched_barrier(0);
      }
    };
    sfor<0, NQ0>(issue);
    __builtin_amdgcn_s_barrier();
    asm volatile("" ::: "memory");
    sfor<NQ0, NQ2>(issue);
    _Pragma("nounroll") for (unsigned spins = 0; spins < (SPIN_CAP << 6); ++spins) {
      if (__builtin_amdgcn_readfirstlane(*(volatile LAS unsigned*)(L + L_Q + 264)) != 0u) break;
      __builtin_amdgcn_s_sleep(1);
    }
    asm volatile("" ::: "memory");
    const v4u q = *(LAS v4u*)(L + L_Q + c * 16);
    {
      const v2u tg = *(LAS v2u*)(L + L_Q + 256);
      tagA = __builtin_amdgcn_readfirstlane(tg.x); tagB = __builtin_amdgcn_readfirstlane(tg.y);
    }
    STAMP(1);
    __builtin_amdgcn_sched_barrier(0);
    float sc[2];
    {
      const bool b3 = c & 8, b2_ = c & 4, b1_ = c & 2, b0 = c & 1;
      float la, lb, lc, ld;
      sfor<0, 32>([&](auto I) {
        constexpr int i = I.value;
        const v4u kk = kv[i];
        float d = dot2(kk.x, q.x, 0.f);
        d = dot2(kk.y, q.y, d); d = dot2(kk.z, q.z, d); d = dot2(kk.w, q.w, d);
        if constexpr ((i & 1) != 0) {
          d = trstep<ROR8>(la, d, b3);
          if constexpr ((i & 2) != 0) {
            d = trstep<XOR7>(lb, d, b2_);
            if constexpr ((i & 4) != 0) {
              d = trstep<XOR2>(lc, d, b1_);
              if constexpr ((i & 8) != 0) d = trstep<XOR1>(ld, d, b0);
            }
          }
        }
        pin1(d);
        if constexpr ((i & 1) == 0) la = d; else if constexpr ((i & 2) == 0) lb = d; else if constexpr ((i & 4) == 0) lc = d;
        else if constexpr ((i & 8) == 0) ld = d; else sc[i >> 4] = d;
        __builtin_amdgcn_sched_barrier(0);
        sfor<kissued(i), kissued(i + 1)>(issue);
      });
    }
    STAMP(2);
    float pr[2];
    {
      const float s0 = rbf(rbf(sc[0]) * 0.08838834764831845f), s1 = rbf(rbf(sc[1]) * 0.08838834764831845f);
      mw = wave_max(fmaxf(s0, s1));
      pr[0] = __builtin_amdgcn_exp2f((s0 - mw) * LOG2E);
      pr[1] = __builtin_amdgcn_exp2f((s1 - mw) * LOG2E);
      const float lw = wave_sum(pr[0] + pr[1]);
      __builtin_amdgcn_raw_buffer_store_b128(v4u{__float_as_uint(mw), tagA, __float_as_uint(lw), tagB}, rs,
                                             lane == 0 ? OFF_LSE + ((bh * 4 + s) * 8 + w) * 16 : OOB, 0, AUX_SC1);
    }
    __builtin_amdgcn_sched_barrier(0);
    sfor<0, 32 - DIST>([&](auto J) {
      constexpr int j = J.value;
      constexpr int c0 = (j * (32 - ENDV) + 31 - DIST) / (32 - DIST), c1 = ((j + 1) * (32 - ENDV) + 31 - DIST) / (32 - DIST);
      sfor<c0, c1>([&](auto I) {
        constexpr int i = I.value;
        axpy8<brev4(i & 15)>(acc, pr[i >> 4], kv[32 + i]);
        pin8(acc);
        __builtin_amdgcn_sched_barrier(0);
      });
      issue(std::integral_constant<int, 32 + j + DIST>{});
      if constexpr (j == 32 - DIST - ENDV - 1) {
        gl = ld128<AUX_SC1>(rs, lse_off, 0);
        __builtin_amdgcn_sched_barrier(0);
      }
    });
    {
      const rsrc_t ro = mkrs(wo + (size_t)(kr * 512 + w * 64) * DM + cg * 16, 64 * DM * 2 - cg * 32);
      wov[0] = ld128<W_AUX>(ro, (lane >> 1) * (DM * 2) + (lane & 1) * 16, 0);
      wov[1] = ld128<W_AUX>(ro, (lane >> 1) * (DM * 2) + (lane & 1) * 16, 32 * DM * 2);
      __builtin_amdgcn_sched_barrier(0);
    }
#pragma unroll
    for (int i = 0; i < ENDV; ++i) ve[i] = kv[64 - ENDV + i];
    pr1 = pr[1];
  } else {
    __builtin_amdgcn_s_setprio(3);
    v4u cpf = zero4;
    if (CPF) {
      unsigned long long pc0; unsigned back, len;
      asm volatile("s_getpc_b64 %0\n.Lfb_here:\n\ts_mov_b32 %1, .Lfb_here - fused_block\n\ts_mov_b32 %2, .Lfb_end - fused_block"
                   : "=s"(pc0), "=s"(back), "=s"(len));
      pc0 -= back;
      cpf = ld128<0>(mkrs((const void*)pc0, (int)len), cg * 1024 + l16, 0);
    }
    const rsrc_t rx = mkrs(x, NB * DM * 2);
    const v2u e = __builtin_amdgcn_raw_buffer_load_b64(rs, OFF_EPOCH, 0, 2);
    const v4u xs0 = ld128<2>(rx, lane * 32, 0), xs1 = ld128<2>(rx, lane * 32 + 16, 0);
    const int xbo = ((c & 1) * DM + 64 * kr + 8 * r) * 2;
    const v4u xb0 = ld128<2>(rx, xbo, 0), xb1 = ld128<2>(rx, xbo + 64, 0);
    v4u wr[16];
    {
      const rsrc_t rq = mkrs(wq + (size_t)(64 * kr) * (NH * DH) + cg * DH, 64 * NH * DH * 2 - cg * DH * 2);
#pragma unroll
      for (int j = 0; j < 16; ++j) wr[j] = ld128<2>(rq, r * (NH * DH * 2) + c * 16, j * 4 * NH * DH * 2);
    }
    *(LAS unsigned*)(L + L_Q + 264) = 0u;
    lds_barrier();
    STAMP(13);
    {
      __builtin_amdgcn_raw_buffer_store_b32(SENT, rs, OFF_OP + (h * 8 + b * 4 + s) * 256 + lane * 4, 0, AUX_SC1);
      __builtin_amdgcn_raw_buffer_store_b64(v2u{SENT, SENT}, rs, ap_st, 0, AUX_SC1);
      const int co = is_m1 ? OFF_PO + (lane >> 3) * PO_CG + mrole * 128 + (lane & 7) * 16 : OOB;
#pragma unroll
      for (int k = 0; k < 4; ++k) __builtin_amdgcn_raw_buffer_store_b128(v4u{SENT, SENT, SENT, SENT}, rs, co, k * 8 * PO_CG, AUX_SC1);
    }
    __builtin_amdgcn_sched_barrier(0);
    v4u xn0, xn1;
    {
      float sm = 0.f;
#pragma unroll
      for (int k = 0; k < 4; ++k) { sm = dot2(xs0[k], 0x3F803F80u, sm); sm = dot2(xs1[k], 0x3F803F80u, sm); }
      const float mean = half_sum(sm) * (1.f / DM);
      float sq = 0.f;
#pragma unroll
      for (int k = 0; k < 4; ++k) {
        const float d0 = lo16(xs0[k]) - mean, d1 = hi16(xs0[k]) - mean, d2 = lo16(xs1[k]) - mean, d3 = hi16(xs1[k]) - mean;
        sq = fmaf(d0, d0, sq); sq = fmaf(d1, d1, sq); sq = fmaf(d2, d2, sq); sq = fmaf(d3, d3, sq);
      }
      const float var = half_sum(sq) * (1.f / DM);
      const float mb = rbf(mean), rd = 1.f / rbf(__builtin_amdgcn_sqrtf(rbf(rbf(var) + 1e-5f)));
      const float m0 = __uint_as_float(__builtin_amdgcn_readlane(__float_as_uint(mb), 0)), m1 = __uint_as_float(__builtin_amdgcn_readlane(__float_as_uint(mb), 32));
      const float r0 = __uint_as_float(__builtin_amdgcn_readlane(__float_as_uint(rd), 0)), r1 = __uint_as_float(__builtin_amdgcn_readlane(__float_as_uint(rd), 32));
      const float mm = (c & 1) ? m1 : m0, rr = (c & 1) ? r1 : r0;
#pragma unroll
      for (int k = 0; k < 4; ++k) {
        const unsigned a0 = pk2(lo16(xb0[k]) - mm, hi16(xb0[k]) - mm), a1 = pk2(lo16(xb1[k]) - mm, hi16(xb1[k]) - mm);
        xn0[k] = pk2(lo16(a0) * rr, hi16(a0) * rr);
        xn1[k] = pk2(lo16(a1) * rr, hi16(a1) * rr);
      }
      const unsigned e0 = __builtin_amdgcn_readfirstlane(e.x), e1 = __builtin_amdgcn_readfirstlane(e.y);
      tagA = e0 + 1u;
      tagB = (e1 ^ (e0 * 0x9E3779B1u)) + 0x7F4A7C15u;
    }
    asm volatile("" : "+v"(xn0), "+v"(xn1) : "v"(cpf));
    STAMP(14);
    {
#pragma unroll
      for (int j = 0; j < 16; ++j) *(LAS v4u*)(L + L_WQ + (4 * j + r) * 272 + c * 16) = wr[j];
      const int ra = L_WQ + (8 * r + (c >> 2)) * 272 + (c & 3) * 8;
      f32x4 d = z4;
#pragma unroll
      for (int n = 0; n < 8; ++n) {
        const f32x4 dn = mfma(trfrag(L + ra + 32 * 272 + n * 32, 4 * 272), xn1, mfma(trfrag(L + ra + n * 32, 4 * 272), xn0, z4));
        if ((c >> 1) == n) d = dn;
      }
      const int qo = OFF_QX + (((cg * 2 + (c & 1)) * 8 + kr) * 64 + (c >> 1) * 8 + 2 * r) * 16;
      __builtin_amdgcn_raw_buffer_store_b128(v4u{__float_as_uint(d.x), tagA, __float_as_uint(d.y), tagB}, rs, qo, 0, AUX_SC1);
      __builtin_amdgcn_raw_buffer_store_b128(v4u{__float_as_uint(d.z), tagA, __float_as_uint(d.w), tagB}, rs, qo + 16, 0, AUX_SC1);
      STAMP(12);
      asm volatile("s_waitcnt vmcnt(0)" ::: "memory");
      __builtin_amdgcn_raw_buffer_store_b64(v2u{tagA, tagB}, rs, lane == 0 ? OFF_FLAG + (kr * 32 + cg) * 8 : OOB, 0, AUX_SC1);
    }
    auto role_tiles = [&](auto W0, auto W1) {
      const rsrc_t r1 = mkrs(w1 + 32 * mrole, DM * FF * 2 - 64 * mrole), r2 = mkrs(w2 + 32 * mrole * DM, 32 * DM * 2);
      const int vo1 = (lane >> 2) * (FF * 2) + (lane & 3) * 16, vo2 = (lane >> 3) * (DM * 2) + (lane & 7) * 16;
      const int wa1 = L_TR + (lane >> 2) * 72 + (lane & 3) * 16, wa2 = L_TR + TR_W2 + (lane >> 3) * 144 + (lane & 7) * 16;
      v4u tv[2][8];
#pragma unroll
      for (int ww = W0.value; ww <= W1.value; ++ww) {
        if (ww < W1.value) {
#pragma unroll
          for (int j = 0; j < 4; ++j) {
            tv[ww & 1][j] = ld128<W_AUX>(r1, vo1, (ww * 64 + j * 16) * FF * 2);
            tv[ww & 1][4 + j] = ld128<W_AUX>(r2, vo2, ww * 128 + j * 8 * DM * 2);
          }
        }
        __builtin_amdgcn_sched_barrier(0);
        if (ww > W0.value) {
#pragma unroll
          for (int j = 0; j < 4; ++j) {
            LAS unsigned char* d = L + wa1 + (ww - 1) * TR_W + j * 16 * 72;
            const v4u v0 = tv[(ww - 1) & 1][j];
            *(LAS v2u*)d = v2u{v0.x, v0.y}; *(LAS v2u*)(d + 8) = v2u{v0.z, v0.w};
            *(LAS v4u*)(L + wa2 + (ww - 1) * TR_W + j * 8 * 144) = tv[(ww - 1) & 1][4 + j];
          }
        }
        __builtin_amdgcn_sched_barrier(0);
      }
    };
    if (RT1 > 0 && is_m1) role_tiles(std::integral_constant<int, 0>{}, std::integral_constant<int, RT1>{});
    {
      const int qx_off = OFF_QX + (h * 2 + b) * 8192 + l16;
      v4u qx[8];
      pace(t0 + QWAIT);
      _Pragma("nounroll") for (unsigned spins = 0; spins < SPIN_CAP; ++spins) {
        asm volatile("" ::: "memory");
        bool bad = false;
#pragma unroll
        for (int k = 0; k < 8; ++k) qx[k] = ld128<AUX_SC1>(rs, qx_off, k * 1024);
#pragma unroll
        for (int k = 0; k < 8; ++k) bad |= (qx[k].y != tagA) | (qx[k].w != tagB);
        if (__builtin_amdgcn_ballot_w64(bad) == 0ull) break;
        __builtin_amdgcn_s_sleep(16);
      }
      float q0 = 0.f, q1 = 0.f;
#pragma unroll
      for (int k = 0; k < 8; ++k) { q0 += __uint_as_float(qx[k].x); q1 += __uint_as_float(qx[k].z); }
      *(LAS unsigned*)(L + L_Q + lane * 4) = pk2(q0, q1);
      *(LAS v2u*)(L + L_Q + 256) = v2u{tagA, tagB};
      asm volatile("s_waitcnt lgkmcnt(0)" ::: "memory");
      *(volatile LAS unsigned*)(L + L_Q + 264) = 1u;
    }
    STAMP(15);
    if (is_m1) {
      role_tiles(std::integral_constant<int, RT1>{}, std::integral_constant<int, 8>{});
      *(LAS float*)(L + L_Q + 272 + (lane & 31) * 4) = lo16(__builtin_amdgcn_raw_buffer_load_b16(mkrs(b1 + 32 * mrole, 64), (lane & 31) * 2, 0, 0));
    }
    if (is_m2) biasf = lo16(__builtin_amdgcn_raw_buffer_load_b16(mkrs(b2 + 16 * mrole, 32), ((lane & 3) * 4 + (lane >> 5) * 2 + (r & 1)) * 2, 0, 0));
    xo = __builtin_amdgcn_raw_buffer_load_b32(rx, kr == 0 ? ((r & 1) * DM + cg * 16 + (c & 14)) * 2 : OOB, 0, 0);
    {
      const int fo0 = OFF_FLAG + ((is_m1 || is_m2) ? l16 : kr * 256 + c * 16), fo1 = (is_m1 || is_m2) ? fo0 + 1024 : fo0;
      pace(t0 + FWAIT + cg * 8);
      _Pragma("nounroll") for (unsigned spins = 0; spins < SPIN_CAP; ++spins) {
        asm volatile("" ::: "memory");
        const v4u fl0 = ld128<AUX_SC1>(rs, fo0, 0), fl1 = ld128<AUX_SC1>(rs, fo1, 0);
        if (__builtin_amdgcn_ballot_w64(fl0.x != tagA || fl0.y != tagB || fl0.z != tagA || fl0.w != tagB ||
                                        fl1.x != tagA || fl1.y != tagB || fl1.z != tagA || fl1.w != tagB) == 0ull) break;
        __builtin_amdgcn_s_sleep(32);
      }
    }
    asm volatile("" : "+v"(xo), "+v"(biasf));
    STAMP(2);
  }

  int pass = DRY ? (w ^ 8) : 1;
  asm volatile("" : "+s"(pass));
  const int tw = (lane >> 1) * 40 + (lane & 1) * 16, tra = (8 * r + (c >> 2)) * 40 + (c & 3) * 8;
#pragma nounroll
  for (;;) {
    const bool doA = w < 8 || !pass, doB = w == 8;
    const rsrc_t rt = mkrs(ws, pass ? WS_BYTES : 0);
    if (doA) {
      if (pass && __builtin_amdgcn_ballot_w64(gl.y != tagA || gl.w != tagB) != 0ull) {
        _Pragma("nounroll") for (unsigned spins = 0; spins < SPIN_CAP; ++spins) {
          asm volatile("" ::: "memory");
          gl = ld128<AUX_SC1>(rs, lse_off, 0);
          if (__builtin_amdgcn_ballot_w64(gl.y != tagA || gl.w != tagB) == 0ull) break;
        }
      }
      const float m_i = __uint_as_float(gl.x), l_i = __uint_as_float(gl.z);
      const float M = wave_max(m_i);
      const float Ls = wave_sum(l_i * __builtin_amdgcn_exp2f((m_i - M) * LOG2E)) * 0.5f;
      float alpha = __builtin_amdgcn_exp2f((mw - M) * LOG2E) / Ls;
      pin1(alpha);
      __builtin_amdgcn_sched_barrier(0);
      sfor<0, ENDV>([&](auto I) {
        constexpr int i = I.value;
        asm volatile("" : "+v"(ve[i]));
        axpy8<brev4((32 - ENDV + i) & 15)>(acc, pr1, ve[i]);
        pin8(acc);
        __builtin_amdgcn_sched_barrier(0);
      });
      STAMP(3);
      const float t0_ = tr32(acc[0], acc[4]), t1 = tr32(acc[1], acc[5]), t2 = tr32(acc[2], acc[6]), t3 = tr32(acc[3], acc[7]);
      f32x2 o2 = {tr16(t0_, t2) * alpha, tr16(t1, t3) * alpha};
      *(LAS f32x2*)(L + L_OP + (w * DH + 8 * c + 2 * r) * 4) = o2;
    }
    lds_barrier_if(pass);
    if (doB) {
      f32x2 o2 = {0.f, 0.f};
#pragma unroll
      for (int ww = 0; ww < 8; ++ww) o2 += *(LAS f32x2*)(L + L_OP + (ww * DH + 2 * lane) * 4);
      __builtin_amdgcn_raw_buffer_store_b32(nosent(pk2(o2.x, o2.y)), rt, OFF_OP + (h * 8 + b * 4 + s) * 256 + lane * 4, 0, AUX_SC1);
      STAMP(4);
    }
    v4u bf[8];
#pragma unroll
    for (int j = 0; j < 8; ++j) bf[j] = zero4;
    float cs = 0.f;
    if (doA) {
      if (is_m1) {
        LAS unsigned char* T = L + L_TR + (w & 7) * TR_W;
#pragma unroll
        for (int j = 0; j < 4; ++j) {
          bf[j] = trfrag(T + (8 * r + (c >> 2)) * 72 + (c & 3) * 8 + (j >> 1) * 32 * 72 + (j & 1) * 32, 288);
          bf[4 + j] = trfrag(T + TR_W2 + (8 * r + (c >> 2)) * 144 + (c & 3) * 8 + j * 32, 576);
        }
        const f32x4 c0 = mfma(ones, bf[2], mfma(ones, bf[0], z4)), c1 = mfma(ones, bf[3], mfma(ones, bf[1], z4));
        cs = r < 2 ? c0.x : c1.x;
        biasf = *(LAS float*)(L + L_Q + 272 + ((r >> 1) * 16 + c) * 4);
        pin1(cs); pin1(biasf);
      }
      __builtin_amdgcn_sched_barrier(0);
      const int po = OFF_OP + (((kr * 4 + ((w & 7) >> 1)) * 8 + (c & 7)) * DH + 64 * (w & 1) + 32 * (c >> 3) + 8 * r) * 2;
      if (D1) __builtin_amdgcn_s_sleep(D1);
      v4u a = ld128<AUX_SC1>(rt, po, 0);
      __builtin_amdgcn_sched_barrier(0);
      LAS unsigned char* T = L + L_WO + w * 2560;
      v4u bo[2];
#pragma unroll
      for (int j = 0; j < 2; ++j) {
        *(LAS v2u*)(T + tw + j * 1280) = v2u{wov[j].x, wov[j].y};
        *(LAS v2u*)(T + tw + j * 1280 + 8) = v2u{wov[j].z, wov[j].w};
        bo[j] = trfrag(T + tra + j * 1280, 160);
      }
      repoll128(a, rt, po);
      STAMP(5);
      const f32x4 d0 = mfma(a, bo[0], z4), d1 = mfma(a, bo[1], z4);
      const f32x4 d = r < 2 ? d0 : d1;
      *(LAS float*)(L + L_P1 + (w * 64 + lane) * 4) = (d.x + d.y) + (d.z + d.w);
    }
    lds_barrier_if(pass);
    if (doB) {
      float u = 0.f;
#pragma unroll
      for (int ww = 0; ww < 8; ++ww) u += *(LAS float*)(L + L_P1 + (ww * 64 + lane) * 4);
      u = tr32(u, u) + ((c & 1) ? hi16(xo) : lo16(xo));
      const unsigned p01 = nosent(pk2(dpp<0x00>(u), dpp<0x55>(u))), p23 = nosent(pk2(dpp<0xAA>(u), dpp<0xFF>(u)));
      __builtin_amdgcn_raw_buffer_store_b64(v2u{p01, p23}, rt, ap_st, 0, AUX_SC1);
      STAMP(6);
    }
    if (is_m1) {
      if (doA) {
        const int ao = OFF_AP + (kr % REPA) * 16384 + (w & 7) * 2048 + c * 64 + r * 16;
        if (D2) __builtin_amdgcn_s_sleep(D2);
        v4u a0 = ld128<AUX_SC1>(rt, ao, 0), a1 = ld128<AUX_SC1>(rt, ao + 1024, 0);
        _Pragma("nounroll") for (unsigned spins = 0; spins < SPIN_CAP; ++spins) {
          const bool bad = has_sent(a0) || has_sent(a1);
          if (__builtin_amdgcn_ballot_w64(bad) == 0ull) break;
          asm volatile("" ::: "memory");
          if (bad) { a0 = ld128<AUX_SC1>(rt, ao, 0); a1 = ld128<AUX_SC1>(rt, ao + 1024, 0); }
        }
        STAMP(7);
        f32x4 d0 = mfma(a0, bf[0], z4), d1 = mfma(a0, bf[1], z4), dg = mfma(a0, a0, z4), dsum = mfma(a0, ones, z4);
        d0 = mfma(a1, bf[2], d0); d1 = mfma(a1, bf[3], d1); dg = mfma(a1, a1, dg); dsum = mfma(a1, ones, dsum);
        const float u = tr16(tr32(d0.x + d0.z, d1.x + d1.z), tr32(d0.y + d0.w, d1.y + d1.w));
        float gq = (c & 1) ? dg.y + dg.w : dg.x + dg.z;
        gq += dpp<ROR2>(gq); gq += dpp<ROR4>(gq); gq += dpp<ROR8>(gq);
        const float sh = (c & 1) ? dsum.y + dsum.w : dsum.x + dsum.z;
        float st = tr32(gq, sh);
        st = tr16(st, st);
        *(LAS f32x2*)(L + L_P2 + (w * 64 + lane) * 8) = f32x2{u, cs};
        if ((lane & 30) == 0) *(LAS float*)(L + L_ST + (w < 8 ? (((c & 1) * 8 + w) * 2 + (r >> 1)) * 4 : 128)) = st;
      }
      lds_barrier_if(pass);
      if (doA) {
        f32x2 uc = {0.f, 0.f};
#pragma unroll
        for (int ww = 0; ww < 8; ++ww) uc += *(LAS f32x2*)(L + L_P2 + (ww * 64 + lane) * 8);
        f32x4 sv = z4;
#pragma unroll
        for (int k = 0; k < 4; ++k) sv += *(LAS f32x4*)(L + L_ST + (r & 1) * 64 + k * 16);
        const float mean = (sv.y + sv.w) * (1.f / DM);
        const float var = fmaxf((sv.x + sv.z) * (1.f / DM) - mean * mean, 0.f);
        const float rd = __builtin_amdgcn_rcpf(rbf(__builtin_amdgcn_sqrtf(rbf(rbf(var) + 1e-5f))));
        const float z = rbf(rbf((uc.x - rbf(mean) * uc.y) * rd) + biasf);
        const float inner = 0.7978845608028654f * (z + 0.044715f * z * z * z);
        const float g = z * __builtin_amdgcn_rcpf(1.f + __builtin_amdgcn_exp2f(-2.f * LOG2E * inner));
        *(LAS u16*)(L + L_GX + w * 128 + (r & 1) * 64 + ((r >> 1) * 16 + c) * 2) = (u16)pk2(g, g);
        const v4u gb = *(LAS v4u*)(L + L_GX + w * 128 + (c & 1) * 64 + r * 16);
        const f32x4 d0 = mfma(bf[4], gb, z4), d1 = mfma(bf[5], gb, z4), d2 = mfma(bf[6], gb, z4), d3 = mfma(bf[7], gb, z4);
        const f32x4 d = (c & 4) ? ((c & 2) ? d3 : d2) : ((c & 2) ? d1 : d0);
        const v4u dv = {nosent(__float_as_uint(d.x)), nosent(__float_as_uint(d.y)), nosent(__float_as_uint(d.z)), nosent(__float_as_uint(d.w))};
        __builtin_amdgcn_raw_buffer_store_b128(dv, rt, c < 8 ? OFF_PO + ((w & 7) * 4 + (c >> 1)) * PO_CG + mrole * 128 + (c & 1) * 64 + r * 16 : OOB, 0, AUX_SC1);
        STAMP(8);
      }
    } else if (is_m2) {
      if (doB) {
        const int ho = OFF_AP + (kr % REPA) * 16384 + ((mrole >> 1) * 16 + (lane >> 2)) * 64 + (mrole & 1) * 32 + (lane & 3) * 8;
        if (D2) __builtin_amdgcn_s_sleep(D2);
        v2u hw;
        _Pragma("nounroll") for (unsigned spins = 0; spins < SPIN_CAP; ++spins) {
          asm volatile("" ::: "memory");
          hw = __builtin_amdgcn_raw_buffer_load_b64(rt, ho, 0, AUX_SC1);
          if (__builtin_amdgcn_ballot_w64(hw.x == SENT || hw.y == SENT) == 0ull) break;
        }
        float hv = tr16(tr32(lo16(hw.x), lo16(hw.y)), tr32(hi16(hw.x), hi16(hw.y)));
        hv = rbf(hv + dpp<ROR8>(hv));
        pin1(hv);
        STAMP(7);
        const int po = OFF_PO + mrole * PO_CG + l16, po5 = lane < 24 ? po : OOB;
        v4u p[6];
        if (D3) __builtin_amdgcn_s_sleep(D3);
        _Pragma("nounroll") for (unsigned spins = 0; spins < SPIN_CAP; ++spins) {
          asm volatile("" ::: "memory");
#pragma unroll
          for (int k = 0; k < 6; ++k) p[k] = ld128<AUX_SC1>(rt, k < 5 ? po : po5, k * 1024);
          bool bad = false;
#pragma unroll
          for (int k = 0; k < 6; ++k) bad |= has_sent(p[k]);
          if (__builtin_amdgcn_ballot_w64(bad) == 0ull) break;
        }
        STAMP(8);
        f32x4 sa = __builtin_bit_cast(f32x4, p[0]);
#pragma unroll
        for (int k = 1; k < 6; ++k) sa += __builtin_bit_cast(f32x4, p[k]);
        float u = tr16(tr32(sa.x, sa.z), tr32(sa.y, sa.w));
        u += dpp<ROR8>(u);
        const float o = hv + rbf(rbf(u) + biasf);
        const rsrc_t ro = mkrs(out, pass ? NB * DM * 2 : 0);
        __builtin_amdgcn_raw_buffer_store_b16((u16)pk2(o, o), ro,
            (lane & 8) == 0 ? (((lane >> 2) & 1) * DM + 16 * mrole + 4 * (lane & 3) + (lane >> 5) * 2 + (r & 1)) * 2 : OOB, 0, 0);
        __builtin_amdgcn_raw_buffer_store_b32(tagA, rt, (mrole == 0 && lane == 0) ? OFF_EPOCH : OOB, 0, AUX_SC1);
        STAMP(9);
      }
    }
    if (pass) break;
    pass = 1;
    asm volatile("" : "+s"(pass));
    __builtin_amdgcn_s_waitcnt(0x0F70);
    STAMP(11);
  }
  asm volatile(".Lfb_end:");
}

extern "C" void kernel_launch(void* const* d_in, const int* in_sizes, int n_in,
                              void* d_out, int out_size, void* d_ws, size_t ws_size,
                              hipStream_t stream) {
  fused_block<<<NWG, NTHR, 0, stream>>>((const u16*)d_in[0], (const u16*)d_in[1], (const u16*)d_in[2],
                                        (const u16*)d_in[3], (const u16*)d_in[4], (const u16*)d_in[6],
                                        (const u16*)d_in[7], (const u16*)d_in[8], (const u16*)d_in[9],
                                        (u16*)d_out, (unsigned char*)d_ws);
}
```

```cpp
#define PROBE 0
#include <hip/hip_runtime.h>
#include <stdint.h>

typedef unsigned short u16;
typedef unsigned v4u __attribute__((ext_vector_type(4)));
typedef unsigned v2u __attribute__((ext_vector_type(2)));
typedef short v4s __attribute__((ext_vector_type(4)));
typedef __bf16 bf16x2 __attribute__((ext_vector_type(2)));
typedef __bf16 bf16x8 __attribute__((ext_vector_type(8)));
typedef float f32x4 __attribute__((ext_vector_type(4)));
typedef float f32x2 __attribute__((ext_vector_type(2)));
typedef __amdgpu_buffer_rsrc_t rsrc_t;
#define GAS __attribute__((address_space(1)))
#define LAS __attribute__((address_space(3)))

#define NQ0 3
#define NQ2 30
#define DIST 6
#define ENDV 5
#define TAU4 160
#define LEAD 2
#define W_AUX 2
#define DRY 1
#define REPA 8
#define D1 0
#define D2 12
#define D3 0
#define CPF 1
#define PFLY 4
#define RT1 2
#define FWAIT 800
#define QWAIT 550

constexpr int NB = 2, DM = 512, NH = 32, SK = 4096, DH = 128, FF = 1376;
constexpr int NWG = 256, NTHR = 576;
constexpr int OFF_EPOCH = 0;
constexpr int OFF_FLAG = 1024;
constexpr int OFF_LSE = 4096;
constexpr int OFF_OP = 65536;
constexpr int OFF_QX = 131072;
constexpr int OFF_PO = OFF_QX + NH * 2 * 8 * 1024;
constexpr int PO_CG = 43 * 128;
constexpr int OFF_AP = OFF_PO + 32 * PO_CG;
constexpr int WS_BYTES = OFF_AP + 8 * 16384;
constexpr int OFF_STAMP = 1 << 20;
constexpr int AUX_SC1 = 16;
constexpr int OOB = 0x7ffffff0;
constexpr unsigned SENT = 0xFFFFFFFFu;
constexpr unsigned SPIN_CAP = 1u << 16;
constexpr int M1_BASE = 0, M1_N = 43;
constexpr int M2_BASE = 64, M2_N = 32;
constexpr int L_Q = 0;
constexpr int L_OP = 512;
constexpr int L_P1 = 5120;
constexpr int L_P2 = 7424;
constexpr int L_ST = 12032;
constexpr int L_WO = 12288;
constexpr int L_WQ = 35328;
constexpr int L_GX = 52736;
constexpr int L_TR = 54272;
constexpr int TR_W = 9216, TR_W2 = 4608;
constexpr int LDS_BYTES = L_TR + 8 * TR_W;

template <int CTRL> __device__ __forceinline__ float dpp(float x) {
  return __builtin_bit_cast(float, __builtin_amdgcn_mov_dpp(__builtin_bit_cast(int, x), CTRL, 0xf, 0xf, true));
}
constexpr int XOR1 = 0xB1, XOR2 = 0x4E, XOR7 = 0x141, ROR1 = 0x121, ROR2 = 0x122, ROR4 = 0x124, ROR8 = 0x128, BCAST = 0x150;
__device__ __forceinline__ float lo16(unsigned v) { return __uint_as_float(v << 16); }
__device__ __forceinline__ float hi16(unsigned v) { return __uint_as_float(v & 0xffff0000u); }
__device__ __forceinline__ unsigned pk2(float a, float b) {
  f32x2 p = {a, b};
  return __builtin_bit_cast(unsigned, __builtin_convertvector(p, bf16x2));
}
__device__ __forceinline__ float rbf(float a) { return lo16(pk2(a, a)); }
__device__ __forceinline__ float dot2(unsigned a, unsigned b, float c) {
  return __builtin_amdgcn_fdot2_f32_bf16(__builtin_bit_cast(bf16x2, a), __builtin_bit_cast(bf16x2, b), c, false);
}
__device__ __forceinline__ f32x4 mfma(v4u a, v4u b, f32x4 c) {
  return __builtin_amdgcn_mfma_f32_16x16x32_bf16(__builtin_bit_cast(bf16x8, a), __builtin_bit_cast(bf16x8, b), c, 0, 0, 0);
}
__device__ __forceinline__ float tr32(float a, float b) {
  auto s = __builtin_amdgcn_permlane32_swap(__float_as_uint(a), __float_as_uint(b), false, false);
  return __uint_as_float(s[0]) + __uint_as_float(s[1]);
}
__device__ __forceinline__ float tr16(float a, float b) {
  auto s = __builtin_amdgcn_permlane16_swap(__float_as_uint(a), __float_as_uint(b), false, false);
  return __uint_as_float(s[0]) + __uint_as_float(s[1]);
}
__device__ __forceinline__ float wave_sum(float x) {
  x += dpp<ROR8>(x); x += dpp<ROR4>(x); x += dpp<ROR2>(x); x += dpp<ROR1>(x);
  x = tr16(x, x); x = tr32(x, x);
  return x;
}
__device__ __forceinline__ float wave_max(float x) {
  x = fmaxf(x, dpp<ROR8>(x)); x = fmaxf(x, dpp<ROR4>(x)); x = fmaxf(x, dpp<ROR2>(x)); x = fmaxf(x, dpp<ROR1>(x));
  auto s = __builtin_amdgcn_permlane16_swap(__float_as_uint(x), __float_as_uint(x), false, false);
  x = fmaxf(__uint_as_float(s[0]), __uint_as_float(s[1]));
  auto t = __builtin_amdgcn_permlane32_swap(__float_as_uint(x), __float_as_uint(x), false, false);
  return fmaxf(__uint_as_float(t[0]), __uint_as_float(t[1]));
}
template <int CTRL> __device__ __forceinline__ float trstep(float lo, float hi, bool bit) {
  const float keep = bit ? hi : lo, send = bit ? lo : hi;
  return keep + dpp<CTRL>(send);
}
__device__ __forceinline__ rsrc_t mkrs(const void* p, int bytes) {
  return __builtin_amdgcn_make_buffer_rsrc((void*)p, 0, bytes, 0x00020000);
}
template <int AUX> __device__ __forceinline__ v4u ld128(rsrc_t rs, int voff, int soff) {
  return __builtin_amdgcn_raw_buffer_load_b128(rs, voff, soff, AUX);
}
__device__ __forceinline__ void lds_barrier() {
  asm volatile("s_waitcnt lgkmcnt(0)" ::: "memory");
  __builtin_amdgcn_s_barrier();
  asm volatile("" ::: "memory");
}
__device__ __forceinline__ void lds_barrier_if(int on) {
  asm volatile("s_waitcnt lgkmcnt(0)\n\ts_cmp_eq_u32 %0, 0\n\ts_cbranch_scc1 1f\n\ts_barrier\n1:" :: "s"(on) : "scc", "memory");
}
__device__ __forceinline__ bool has_sent(v4u v) { return (v.x == SENT) | (v.y == SENT) | (v.z == SENT) | (v.w == SENT); }
__device__ __forceinline__ unsigned nosent(unsigned v) { return v < SENT ? v : SENT - 1u; }
constexpr int brev4(int i) { return ((i & 1) << 3) | ((i & 2) << 1) | ((i & 4) >> 1) | ((i & 8) >> 3); }
__device__ __forceinline__ void pace(unsigned target) {
  while ((int)((unsigned)__builtin_amdgcn_s_memrealtime() - target) < 0) __builtin_amdgcn_s_sleep(1);
}
__device__ __forceinline__ v4u trfrag(LAS unsigned char* p, int rs4) {
  const v2u u0 = __builtin_bit_cast(v2u, __builtin_amdgcn_ds_read_tr16_b64_v4i16((LAS v4s*)p));
  const v2u u1 = __builtin_bit_cast(v2u, __builtin_amdgcn_ds_read_tr16_b64_v4i16((LAS v4s*)(p + rs4)));
  return v4u{u0.x, u0.y, u1.x, u1.y};
}
__device__ __forceinline__ void repoll128(v4u& a, rsrc_t rs, int off) {
  _Pragma("nounroll") for (unsigned spins = 0; spins < SPIN_CAP; ++spins) {
    const bool bad = has_sent(a);
    if (__builtin_amdgcn_ballot_w64(bad) == 0ull) break;
    asm volatile("" : "+v"(off) :: "memory");
    if (bad) a = ld128<AUX_SC1>(rs, off, 0);
  }
}
__device__ __forceinline__ float half_sum(float x) {
  x += dpp<ROR8>(x); x += dpp<ROR4>(x); x += dpp<ROR2>(x); x += dpp<ROR1>(x);
  return tr16(x, x);
}

template <int I> __device__ __forceinline__ void axpy8(float (&acc)[8], float pv, v4u v) {
  const float p = dpp<BCAST + I>(pv);
#pragma unroll
  for (int k = 0; k < 4; ++k) {
    acc[2 * k] = fmaf(p, lo16(v[k]), acc[2 * k]);
    acc[2 * k + 1] = fmaf(p, hi16(v[k]), acc[2 * k + 1]);
  }
}
__device__ __forceinline__ void pin8(float (&a)[8]) {
  asm volatile("" : "+v"(a[0]), "+v"(a[1]), "+v"(a[2]), "+v"(a[3]), "+v"(a[4]), "+v"(a[5]), "+v"(a[6]), "+v"(a[7]));
}
__device__ __forceinline__ void pin1(float& a) { asm volatile("" : "+v"(a)); }
constexpr int kissued(int i) { return (NQ2 + (i + 3) / 4 > i + DIST) ? NQ2 + (i + 3) / 4 : i + DIST; }
static_assert(kissued(32) == 32 + DIST, "the distance is reached inside the K phase");
template <int I, int N, typename F> __device__ __forceinline__ void sfor(F&& f) {
  if constexpr (I < N) { f(std::integral_constant<int, I>{}); sfor<I + 1, N>(f); }
}

#define STAMP(i) do { if ((i) == PROBE) tprobe = (unsigned)__builtin_amdgcn_s_memrealtime(); } while (0)

extern "C" __global__ void __launch_bounds__(NTHR)
fused_block(const u16* __restrict__ x, const u16* __restrict__ kc, const u16* __restrict__ vc,
            const u16* __restrict__ wq, const u16* __restrict__ wo, const u16* __restrict__ w1,
            const u16* __restrict__ b1, const u16* __restrict__ w2, const u16* __restrict__ b2,
            u16* out, unsigned char* ws) {
  __shared__ __attribute__((aligned(16))) unsigned char smem[LDS_BYTES];
  LAS unsigned char* L = (LAS unsigned char*)smem;
  const unsigned t0 = (unsigned)__builtin_amdgcn_s_memrealtime();
  const int tid = threadIdx.x, lane = tid & 63, w = (__builtin_amdgcn_readfirstlane(tid >> 6) + 8) % 9;
  const int r = lane >> 4, c = lane & 15;
  const int bid = blockIdx.x;
  const int kr = bid & 7, cg = bid >> 3;
  const int h = kr * 4 + (cg & 3), b = bid >> 7, s = (bid >> 5) & 3;
  const int bh = b * NH + h;
  const int l16 = lane * 16;
  const rsrc_t rs = mkrs(ws, WS_BYTES);
  const bool is_m1 = cg < 6 && kr * 6 + cg < M1_N;
  const bool is_m2 = bid >= M2_BASE && bid < M2_BASE + M2_N;
  const int mrole = is_m1 ? kr * 6 + cg : bid - M2_BASE;
  constexpr float LOG2E = 1.4426950408889634f;
  const f32x4 z4 = {0.f, 0.f, 0.f, 0.f};
  const v4u zero4 = {0u, 0u, 0u, 0u};
  const v4u ones = {0x3F803F80u, 0x3F803F80u, 0x3F803F80u, 0x3F803F80u};
  unsigned tprobe = t0;
  v4u ve[ENDV], gl = zero4, wov[2] = {zero4, zero4};
#pragma unroll
  for (int i = 0; i < ENDV; ++i) ve[i] = zero4;
  float acc[8], pr1 = 0.f, mw = 0.f, biasf = 0.f;
#pragma unroll
  for (int k = 0; k < 8; ++k) acc[k] = 0.f;
  unsigned tagA, tagB, xo = 0u;
  const int lse_off = OFF_LSE + (bh * 32 + (lane & 31)) * 16;
  const int ap_rep = (c & 3) + 4 * (r >> 1);
  const int ap_st = ap_rep < REPA ? OFF_AP + ap_rep * 16384 + ((cg >> 1) * 16 + kr * 2 + (r & 1)) * 64 + (cg & 1) * 32 + (c >> 2) * 8 : OOB;

  if (w < 8) {
    v4u kv[64];
    const size_t kvoff = ((size_t)bh * SK + s * 1024 + w * 128) * DH;
    const rsrc_t rk = mkrs(kc + kvoff, 32768), rv = mkrs(vc + kvoff, 32768);
    auto issue = [&](auto N) {
      constexpr int n = N.value;
      if constexpr (n < 64) {
        if constexpr (TAU4 > 0 && n >= NQ0 && ((n - NQ0) & 1) == 0) pace(t0 + (unsigned)((n - LEAD) * TAU4 / 4));
        if constexpr (n >= PFLY && n < kissued(32 - DIST)) asm volatile("s_waitcnt vmcnt(%0)" :: "i"(PFLY - 1));
        if constexpr (n < 32) kv[n] = ld128<2>(rk, l16, n * 1024); else kv[n] = ld128<2>(rv, l16, (n - 32) * 1024);
        __builtin_amdgcn_sched_barrier(0);
      }
    };
    sfor<0, NQ0>(issue);
    __builtin_amdgcn_s_barrier();
    asm volatile("" ::: "memory");
    sfor<NQ0, NQ2>(issue);
    _Pragma("nounroll") for (unsigned spins = 0; spins < (SPIN_CAP << 6); ++spins) {
      if (__builtin_amdgcn_readfirstlane(*(volatile LAS unsigned*)(L + L_Q + 264)) != 0u) break;
      __builtin_amdgcn_s_sleep(1);
    }
    asm volatile("" ::: "memory");
    const v4u q = *(LAS v4u*)(L + L_Q + c * 16);
    {
      const v2u tg = *(LAS v2u*)(L + L_Q + 256);
      tagA = __builtin_amdgcn_readfirstlane(tg.x); tagB = __builtin_amdgcn_readfirstlane(tg.y);
    }
    STAMP(1);
    __builtin_amdgcn_sched_barrier(0);
    float sc[2];
    {
      const bool b3 = c & 8, b2_ = c & 4, b1_ = c & 2, b0 = c & 1;
      float la, lb, lc, ld;
      sfor<0, 32>([&](auto I) {
        constexpr int i = I.value;
        const v4u kk = kv[i];
        float d = dot2(kk.x, q.x, 0.f);
        d = dot2(kk.y, q.y, d); d = dot2(kk.z, q.z, d); d = dot2(kk.w, q.w, d);
        if constexpr ((i & 1) != 0) {
          d = trstep<ROR8>(la, d, b3);
          if constexpr ((i & 2) != 0) {
            d = trstep<XOR7>(lb, d, b2_);
            if constexpr ((i & 4) != 0) {
              d = trstep<XOR2>(lc, d, b1_);
              if constexpr ((i & 8) != 0) d = trstep<XOR1>(ld, d, b0);
            }
          }
        }
        pin1(d);
        if constexpr ((i & 1) == 0) la = d; else if constexpr ((i & 2) == 0) lb = d; else if constexpr ((i & 4) == 0) lc = d;
        else if constexpr ((i & 8) == 0) ld = d; else sc[i >> 4] = d;
        __builtin_amdgcn_sched_barrier(0);
        sfor<kissued(i), kissued(i + 1)>(issue);
      });
    }
    STAMP(2);
    float pr[2];
    {
      const float s0 = rbf(rbf(sc[0]) * 0.08838834764831845f), s1 = rbf(rbf(sc[1]) * 0.08838834764831845f);
      mw = wave_max(fmaxf(s0, s1));
      pr[0] = __builtin_amdgcn_exp2f((s0 - mw) * LOG2E);
      pr[1] = __builtin_amdgcn_exp2f((s1 - mw) * LOG2E);
      const float lw = wave_sum(pr[0] + pr[1]);
      __builtin_amdgcn_raw_buffer_store_b128(v4u{__float_as_uint(mw), tagA, __float_as_uint(lw), tagB}, rs,
                                             lane == 0 ? OFF_LSE + ((bh * 4 + s) * 8 + w) * 16 : OOB, 0, AUX_SC1);
    }
    __builtin_amdgcn_sched_barrier(0);
    sfor<0, 32 - DIST>([&](auto J) {
      constexpr int j = J.value;
      constexpr int c0 = (j * (32 - ENDV) + 31 - DIST) / (32 - DIST), c1 = ((j + 1) * (32 - ENDV) + 31 - DIST) / (32 - DIST);
      sfor<c0, c1>([&](auto I) {
        constexpr int i = I.value;
        axpy8<brev4(i & 15)>(acc, pr[i >> 4], kv[32 + i]);
        pin8(acc);
        __builtin_amdgcn_sched_barrier(0);
      });
      issue(std::integral_constant<int, 32 + j + DIST>{});
      if constexpr (j == 32 - DIST - ENDV - 1) {
        gl = ld128<AUX_SC1>(rs, lse_off, 0);
        __builtin_amdgcn_sched_barrier(0);
      }
    });
    {
      const rsrc_t ro = mkrs(wo + (size_t)(kr * 512 + w * 64) * DM + cg * 16, 64 * DM * 2 - cg * 32);
      wov[0] = ld128<W_AUX>(ro, (lane >> 1) * (DM * 2) + (lane & 1) * 16, 0);
      wov[1] = ld128<W_AUX>(ro, (lane >> 1) * (DM * 2) + (lane & 1) * 16, 32 * DM * 2);
      __builtin_amdgcn_sched_barrier(0);
    }
#pragma unroll
    for (int i = 0; i < ENDV; ++i) ve[i] = kv[64 - ENDV + i];
    pr1 = pr[1];
  } else {
    __builtin_amdgcn_s_setprio(3);
    v4u cpf = zero4;
    if (CPF) {
      unsigned long long pc0; unsigned back, len;
      asm volatile("s_getpc_b64 %0\n.Lfb_here:\n\ts_mov_b32 %1, .Lfb_here - fused_block\n\ts_mov_b32 %2, .Lfb_end - fused_block"
                   : "=s"(pc0), "=s"(back), "=s"(len));
      pc0 -= back;
      cpf = ld128<0>(mkrs((const void*)pc0, (int)len), cg * 1024 + l16, 0);
    }
    const rsrc_t rx = mkrs(x, NB * DM * 2);
    const v2u e = __builtin_amdgcn_raw_buffer_load_b64(rs, OFF_EPOCH, 0, 2);
    const v4u xs0 = ld128<2>(rx, lane * 32, 0), xs1 = ld128<2>(rx, lane * 32 + 16, 0);
    const int xbo = ((c & 1) * DM + 64 * kr + 8 * r) * 2;
    const v4u xb0 = ld128<2>(rx, xbo, 0), xb1 = ld128<2>(rx, xbo + 64, 0);
    v4u wr[16];
    {
      const rsrc_t rq = mkrs(wq + (size_t)(64 * kr) * (NH * DH) + cg * DH, 64 * NH * DH * 2 - cg * DH * 2);
#pragma unroll
      for (int j = 0; j < 16; ++j) wr[j] = ld128<2>(rq, r * (NH * DH * 2) + c * 16, j * 4 * NH * DH * 2);
    }
    *(LAS unsigned*)(L + L_Q + 264) = 0u;
    lds_barrier();
    STAMP(13);
    {
      __builtin_amdgcn_raw_buffer_store_b32(SENT, rs, OFF_OP + (h * 8 + b * 4 + s) * 256 + lane * 4, 0, AUX_SC1);
      __builtin_amdgcn_raw_buffer_store_b64(v2u{SENT, SENT}, rs, ap_st, 0, AUX_SC1);
      const int co = is_m1 ? OFF_PO + (lane >> 3) * PO_CG + mrole * 128 + (lane & 7) * 16 : OOB;
#pragma unroll
      for (int k = 0; k < 4; ++k) __builtin_amdgcn_raw_buffer_store_b128(v4u{SENT, SENT, SENT, SENT}, rs, co, k * 8 * PO_CG, AUX_SC1);
    }
    __builtin_amdgcn_sched_barrier(0);
    v4u xn0, xn1;
    {
      float sm = 0.f;
#pragma unroll
      for (int k = 0; k < 4; ++k) { sm = dot2(xs0[k], 0x3F803F80u, sm); sm = dot2(xs1[k], 0x3F803F80u, sm); }
      const float mean = half_sum(sm) * (1.f / DM);
      float sq = 0.f;
#pragma unroll
      for (int k = 0; k < 4; ++k) {
        const float d0 = lo16(xs0[k]) - mean, d1 = hi16(xs0[k]) - mean, d2 = lo16(xs1[k]) - mean, d3 = hi16(xs1[k]) - mean;
        sq = fmaf(d0, d0, sq); sq = fmaf(d1, d1, sq); sq = fmaf(d2, d2, sq); sq = fmaf(d3, d3, sq);
      }
      const float var = half_sum(sq) * (1.f / DM);
      const float mb = rbf(mean), rd = 1.f / rbf(__builtin_amdgcn_sqrtf(rbf(rbf(var) + 1e-5f)));
      const float m0 = __uint_as_float(__builtin_amdgcn_readlane(__float_as_uint(mb), 0)), m1 = __uint_as_float(__builtin_amdgcn_readlane(__float_as_uint(mb), 32));
      const float r0 = __uint_as_float(__builtin_amdgcn_readlane(__float_as_uint(rd), 0)), r1 = __uint_as_float(__builtin_amdgcn_readlane(__float_as_uint(rd), 32));
      const float mm = (c & 1) ? m1 : m0, rr = (c & 1) ? r1 : r0;
#pragma unroll
      for (int k = 0; k < 4; ++k) {
        const unsigned a0 = pk2(lo16(xb0[k]) - mm, hi16(xb0[k]) - mm), a1 = pk2(lo16(xb1[k]) - mm, hi16(xb1[k]) - mm);
        xn0[k] = pk2(lo16(a0) * rr, hi16(a0) * rr);
        xn1[k] = pk2(lo16(a1) * rr, hi16(a1) * rr);
      }
      const unsigned e0 = __builtin_amdgcn_readfirstlane(e.x), e1 = __builtin_amdgcn_readfirstlane(e.y);
      tagA = e0 + 1u;
      tagB = (e1 ^ (e0 * 0x9E3779B1u)) + 0x7F4A7C15u;
    }
    asm volatile("" : "+v"(xn0), "+v"(xn1) : "v"(cpf));
    STAMP(14);
    {
#pragma unroll
      for (int j = 0; j < 16; ++j) *(LAS v4u*)(L + L_WQ + (4 * j + r) * 272 + c * 16) = wr[j];
      const int ra = L_WQ + (8 * r + (c >> 2)) * 272 + (c & 3) * 8;
      f32x4 d = z4;
#pragma unroll
      for (int n = 0; n < 8; ++n) {
        const f32x4 dn = mfma(trfrag(L + ra + 32 * 272 + n * 32, 4 * 272), xn1, mfma(trfrag(L + ra + n * 32, 4 * 272), xn0, z4));
        if ((c >> 1) == n) d = dn;
      }
      const int qo = OFF_QX + (((cg * 2 + (c & 1)) * 8 + kr) * 64 + (c >> 1) * 8 + 2 * r) * 16;
      __builtin_amdgcn_raw_buffer_store_b128(v4u{__float_as_uint(d.x), tagA, __float_as_uint(d.y), tagB}, rs, qo, 0, AUX_SC1);
      __builtin_amdgcn_raw_buffer_store_b128(v4u{__float_as_uint(d.z), tagA, __float_as_uint(d.w), tagB}, rs, qo + 16, 0, AUX_SC1);
      STAMP(12);
      asm volatile("s_waitcnt vmcnt(0)" ::: "memory");
      __builtin_amdgcn_raw_buffer_store_b64(v2u{tagA, tagB}, rs, lane == 0 ? OFF_FLAG + (kr * 32 + cg) * 8 : OOB, 0, AUX_SC1);
    }
    auto role_tiles = [&](auto W0, auto W1) {
      const rsrc_t r1 = mkrs(w1 + 32 * mrole, DM * FF * 2 - 64 * mrole), r2 = mkrs(w2 + 32 * mrole * DM, 32 * DM * 2);
      const int vo1 = (lane >> 2) * (FF * 2) + (lane & 3) * 16, vo2 = (lane >> 3) * (DM * 2) + (lane & 7) * 16;
      const int wa1 = L_TR + (lane >> 2) * 72 + (lane & 3) * 16, wa2 = L_TR + TR_W2 + (lane >> 3) * 144 + (lane & 7) * 16;
      v4u tv[2][8];
#pragma unroll
      for (int ww = W0.value; ww <= W1.value; ++ww) {
        if (ww < W1.value) {
#pragma unroll
          for (int j = 0; j < 4; ++j) {
            tv[ww & 1][j] = ld128<W_AUX>(r1, vo1, (ww * 64 + j * 16) * FF * 2);
            tv[ww & 1][4 + j] = ld128<W_AUX>(r2, vo2, ww * 128 + j * 8 * DM * 2);
          }
        }
        __builtin_amdgcn_sched_barrier(0);
        if (ww > W0.value) {
#pragma unroll
          for (int j = 0; j < 4; ++j) {
            LAS unsigned char* d = L + wa1 + (ww - 1) * TR_W + j * 16 * 72;
            const v4u v0 = tv[(ww - 1) & 1][j];
            *(LAS v2u*)d = v2u{v0.x, v0.y}; *(LAS v2u*)(d + 8) = v2u{v0.z, v0.w};
            *(LAS v4u*)(L + wa2 + (ww - 1) * TR_W + j * 8 * 144) = tv[(ww - 1) & 1][4 + j];
          }
        }
        __builtin_amdgcn_sched_barrier(0);
      }
    };
    if (RT1 > 0 && is_m1) role_tiles(std::integral_constant<int, 0>{}, std::integral_constant<int, RT1>{});
    {
      const int qx_off = OFF_QX + (h * 2 + b) * 8192 + l16;
      v4u qx[8];
      pace(t0 + QWAIT);
      _Pragma("nounroll") for (unsigned spins = 0; spins < SPIN_CAP; ++spins) {
        asm volatile("" ::: "memory");
        bool bad = false;
#pragma unroll
        for (int k = 0; k < 8; ++k) qx[k] = ld128<AUX_SC1>(rs, qx_off, k * 1024);
#pragma unroll
        for (int k = 0; k < 8; ++k) bad |= (qx[k].y != tagA) | (qx[k].w != tagB);
        if (__builtin_amdgcn_ballot_w64(bad) == 0ull) break;
        __builtin_amdgcn_s_sleep(16);
      }
      float q0 = 0.f, q1 = 0.f;
#pragma unroll
      for (int k = 0; k < 8; ++k) { q0 += __uint_as_float(qx[k].x); q1 += __uint_as_float(qx[k].z); }
      *(LAS unsigned*)(L + L_Q + lane * 4) = pk2(q0, q1);
      *(LAS v2u*)(L + L_Q + 256) = v2u{tagA, tagB};
      asm volatile("s_waitcnt lgkmcnt(0)" ::: "memory");
      *(volatile LAS unsigned*)(L + L_Q + 264) = 1u;
    }
    STAMP(15);
    if (is_m1) {
      role_tiles(std::integral_constant<int, RT1>{}, std::integral_constant<int, 8>{});
      *(LAS float*)(L + L_Q + 272 + (lane & 31) * 4) = lo16(__builtin_amdgcn_raw_buffer_load_b16(mkrs(b1 + 32 * mrole, 64), (lane & 31) * 2, 0, 0));
    }
    if (is_m2) biasf = lo16(__builtin_amdgcn_raw_buffer_load_b16(mkrs(b2 + 16 * mrole, 32), ((lane & 3) * 4 + (lane >> 5) * 2 + (r & 1)) * 2, 0, 0));
    xo = __builtin_amdgcn_raw_buffer_load_b32(rx, kr == 0 ? ((r & 1) * DM + cg * 16 + (c & 14)) * 2 : OOB, 0, 0);
    {
      const int fo0 = OFF_FLAG + ((is_m1 || is_m2) ? l16 : kr * 256 + c * 16), fo1 = (is_m1 || is_m2) ? fo0 + 1024 : fo0;
      pace(t0 + FWAIT + cg * 8);
      _Pragma("nounroll") for (unsigned spins = 0; spins < SPIN_CAP; ++spins) {
        asm volatile("" ::: "memory");
        const v4u fl0 = ld128<AUX_SC1>(rs, fo0, 0), fl1 = ld128<AUX_SC1>(rs, fo1, 0);
        if (__builtin_amdgcn_ballot_w64(fl0.x != tagA || fl0.y != tagB || fl0.z != tagA || fl0.w != tagB ||
                                        fl1.x != tagA || fl1.y != tagB || fl1.z != tagA || fl1.w != tagB) == 0ull) break;
        __builtin_amdgcn_s_sleep(32);
      }
    }
    asm volatile("" : "+v"(xo), "+v"(biasf));
    STAMP(2);
  }

  int pass = DRY ? (w ^ 8) : 1;
  asm volatile("" : "+s"(pass));
  const int tw = (lane >> 1) * 40 + (lane & 1) * 16, tra = (8 * r + (c >> 2)) * 40 + (c & 3) * 8;
#pragma nounroll
  for (;;) {
    const bool doA = w < 8 || !pass, doB = w == 8;
    const rsrc_t rt = mkrs(ws, pass ? WS_BYTES : 0);
    if (doA) {
      if (pass && __builtin_amdgcn_ballot_w64(gl.y != tagA || gl.w != tagB) != 0ull) {
        _Pragma("nounroll") for (unsigned spins = 0; spins < SPIN_CAP; ++spins) {
          asm volatile("" ::: "memory");
          gl = ld128<AUX_SC1>(rs, lse_off, 0);
          if (__builtin_amdgcn_ballot_w64(gl.y != tagA || gl.w != tagB) == 0ull) break;
        }
      }
      const float m_i = __uint_as_float(gl.x), l_i = __uint_as_float(gl.z);
      const float M = wave_max(m_i);
      const float Ls = wave_sum(l_i * __builtin_amdgcn_exp2f((m_i - M) * LOG2E)) * 0.5f;
      float alpha = __builtin_amdgcn_exp2f((mw - M) * LOG2E) / Ls;
      pin1(alpha);
      __builtin_amdgcn_sched_barrier(0);
      sfor<0, ENDV>([&](auto I) {
        constexpr int i = I.value;
        asm volatile("" : "+v"(ve[i]));
        axpy8<brev4((32 - ENDV + i) & 15)>(acc, pr1, ve[i]);
        pin8(acc);
        __builtin_amdgcn_sched_barrier(0);
      });
      STAMP(3);
      const float t0_ = tr32(acc[0], acc[4]), t1 = tr32(acc[1], acc[5]), t2 = tr32(acc[2], acc[6]), t3 = tr32(acc[3], acc[7]);
      f32x2 o2 = {tr16(t0_, t2) * alpha, tr16(t1, t3) * alpha};
      *(LAS f32x2*)(L + L_OP + (w * DH + 8 * c + 2 * r) * 4) = o2;
    }
    lds_barrier_if(pass);
    if (doB) {
      f32x2 o2 = {0.f, 0.f};
#pragma unroll
      for (int ww = 0; ww < 8; ++ww) o2 += *(LAS f32x2*)(L + L_OP + (ww * DH + 2 * lane) * 4);
      __builtin_amdgcn_raw_buffer_store_b32(nosent(pk2(o2.x, o2.y)), rt, OFF_OP + (h * 8 + b * 4 + s) * 256 + lane * 4, 0, AUX_SC1);
      STAMP(4);
    }
    v4u bf[8];
#pragma unroll
    for (int j = 0; j < 8; ++j) bf[j] = zero4;
    float cs = 0.f;
    if (doA) {
      if (is_m1) {
        LAS unsigned char* T = L + L_TR + (w & 7) * TR_W;
#pragma unroll
        for (int j = 0; j < 4; ++j) {
          bf[j] = trfrag(T + (8 * r + (c >> 2)) * 72 + (c & 3) * 8 + (j >> 1) * 32 * 72 + (j & 1) * 32, 288);
          bf[4 + j] = trfrag(T + TR_W2 + (8 * r + (c >> 2)) * 144 + (c & 3) * 8 + j * 32, 576);
        }
        const f32x4 c0 = mfma(ones, bf[2], mfma(ones, bf[0], z4)), c1 = mfma(ones, bf[3], mfma(ones, bf[1], z4));
        cs = r < 2 ? c0.x : c1.x;
        biasf = *(LAS float*)(L + L_Q + 272 + ((r >> 1) * 16 + c) * 4);
        pin1(cs); pin1(biasf);
      }
      __builtin_amdgcn_sched_barrier(0);
      const int po = OFF_OP + (((kr * 4 + ((w & 7) >> 1)) * 8 + (c & 7)) * DH + 64 * (w & 1) + 32 * (c >> 3) + 8 * r) * 2;
      if (D1) __builtin_amdgcn_s_sleep(D1);
      v4u a = ld128<AUX_SC1>(rt, po, 0);
      __builtin_amdgcn_sched_barrier(0);
      LAS unsigned char* T = L + L_WO + w * 2560;
      v4u bo[2];
#pragma unroll
      for (int j = 0; j < 2; ++j) {
        *(LAS v2u*)(T + tw + j * 1280) = v2u{wov[j].x, wov[j].y};
        *(LAS v2u*)(T + tw + j * 1280 + 8) = v2u{wov[j].z, wov[j].w};
        bo[j] = trfrag(T + tra + j * 1280, 160);
      }
      repoll128(a, rt, po);
      STAMP(5);
      const f32x4 d0 = mfma(a, bo[0], z4), d1 = mfma(a, bo[1], z4);
      const f32x4 d = r < 2 ? d0 : d1;
      *(LAS float*)(L + L_P1 + (w * 64 + lane) * 4) = (d.x + d.y) + (d.z + d.w);
    }
    lds_barrier_if(pass);
    if (doB) {
      float u = 0.f;
#pragma unroll
      for (int ww = 0; ww < 8; ++ww) u += *(LAS float*)(L + L_P1 + (ww * 64 + lane) * 4);
      u = tr32(u, u) + ((c & 1) ? hi16(xo) : lo16(xo));
      const unsigned p01 = nosent(pk2(dpp<0x00>(u), dpp<0x55>(u))), p23 = nosent(pk2(dpp<0xAA>(u), dpp<0xFF>(u)));
      __builtin_amdgcn_raw_buffer_store_b64(v2u{p01, p23}, rt, ap_st, 0, AUX_SC1);
      STAMP(6);
    }
    if (is_m1) {
      if (doA) {
        const int ao = OFF_AP + (kr % REPA) * 16384 + (w & 7) * 2048 + c * 64 + r * 16;
        if (D2) __builtin_amdgcn_s_sleep(D2);
        v4u a0 = ld128<AUX_SC1>(rt, ao, 0), a1 = ld128<AUX_SC1>(rt, ao + 1024, 0);
        _Pragma("nounroll") for (unsigned spins = 0; spins < SPIN_CAP; ++spins) {
          const bool bad = has_sent(a0) || has_sent(a1);
          if (__builtin_amdgcn_ballot_w64(bad) == 0ull) break;
          asm volatile("" ::: "memory");
          if (bad) { a0 = ld128<AUX_SC1>(rt, ao, 0); a1 = ld128<AUX_SC1>(rt, ao + 1024, 0); }
        }
        STAMP(7);
        f32x4 d0 = mfma(a0, bf[0], z4), d1 = mfma(a0, bf[1], z4), dg = mfma(a0, a0, z4), dsum = mfma(a0, ones, z4);
        d0 = mfma(a1, bf[2], d0); d1 = mfma(a1, bf[3], d1); dg = mfma(a1, a1, dg); dsum = mfma(a1, ones, dsum);
        const float u = tr16(tr32(d0.x + d0.z, d1.x + d1.z), tr32(d0.y + d0.w, d1.y + d1.w));
        float gq = (c & 1) ? dg.y + dg.w : dg.x + dg.z;
        gq += dpp<ROR2>(gq); gq += dpp<ROR4>(gq); gq += dpp<ROR8>(gq);
        const float sh = (c & 1) ? dsum.y + dsum.w : dsum.x + dsum.z;
        float st = tr32(gq, sh);
        st = tr16(st, st);
        *(LAS f32x2*)(L + L_P2 + (w * 64 + lane) * 8) = f32x2{u, cs};
        if ((lane & 30) == 0) *(LAS float*)(L + L_ST + (w < 8 ? (((c & 1) * 8 + w) * 2 + (r >> 1)) * 4 : 128)) = st;
      }
      lds_barrier_if(pass);
      if (doA) {
        f32x2 uc = {0.f, 0.f};
#pragma unroll
        for (int ww = 0; ww < 8; ++ww) uc += *(LAS f32x2*)(L + L_P2 + (ww * 64 + lane) * 8);
        f32x4 sv = z4;
#pragma unroll
        for (int k = 0; k < 4; ++k) sv += *(LAS f32x4*)(L + L_ST + (r & 1) * 64 + k * 16);
        const float mean = (sv.y + sv.w) * (1.f / DM);
        const float var = fmaxf((sv.x + sv.z) * (1.f / DM) - mean * mean, 0.f);
        const float rd = __builtin_amdgcn_rcpf(rbf(__builtin_amdgcn_sqrtf(rbf(rbf(var) + 1e-5f))));
        const float z = rbf(rbf((uc.x - rbf(mean) * uc.y) * rd) + biasf);
        const float inner = 0.7978845608028654f * (z + 0.044715f * z * z * z);
        const float g = z * __builtin_amdgcn_rcpf(1.f + __builtin_amdgcn_exp2f(-2.f * LOG2E * inner));
        *(LAS u16*)(L + L_GX + w * 128 + (r & 1) * 64 + ((r >> 1) * 16 + c) * 2) = (u16)pk2(g, g);
        const v4u gb = *(LAS v4u*)(L + L_GX + w * 128 + (c & 1) * 64 + r * 16);
        const f32x4 d0 = mfma(bf[4], gb, z4), d1 = mfma(bf[5], gb, z4), d2 = mfma(bf[6], gb, z4), d3 = mfma(bf[7], gb, z4);
        const f32x4 d = (c & 4) ? ((c & 2) ? d3 : d2) : ((c & 2) ? d1 : d0);
        const v4u dv = {nosent(__float_as_uint(d.x)), nosent(__float_as_uint(d.y)), nosent(__float_as_uint(d.z)), nosent(__float_as_uint(d.w))};
        __builtin_amdgcn_raw_buffer_store_b128(dv, rt, c < 8 ? OFF_PO + ((w & 7) * 4 + (c >> 1)) * PO_CG + mrole * 128 + (c & 1) * 64 + r * 16 : OOB, 0, AUX_SC1);
        STAMP(8);
      }
    } else if (is_m2) {
      if (doB) {
        const int ho = OFF_AP + (kr % REPA) * 16384 + ((mrole >> 1) * 16 + (lane >> 2)) * 64 + (mrole & 1) * 32 + (lane & 3) * 8;
        if (D2) __builtin_amdgcn_s_sleep(D2);
        v2u hw;
        _Pragma("nounroll") for (unsigned spins = 0; spins < SPIN_CAP; ++spins) {
          asm volatile("" ::: "memory");
          hw = __builtin_amdgcn_raw_buffer_load_b64(rt, ho, 0, AUX_SC1);
          if (__builtin_amdgcn_ballot_w64(hw.x == SENT || hw.y == SENT) == 0ull) break;
        }
        float hv = tr16(tr32(lo16(hw.x), lo16(hw.y)), tr32(hi16(hw.x), hi16(hw.y)));
        hv = rbf(hv + dpp<ROR8>(hv));
        pin1(hv);
        STAMP(7);
        const int po = OFF_PO + mrole * PO_CG + l16, po5 = lane < 24 ? po : OOB;
        v4u p[6];
        if (D3) __builtin_amdgcn_s_sleep(D3);
        _Pragma("nounroll") for (unsigned spins = 0; spins < SPIN_CAP; ++spins) {
          asm volatile("" ::: "memory");
#pragma unroll
          for (int k = 0; k < 6; ++k) p[k] = ld128<AUX_SC1>(rt, k < 5 ? po : po5, k * 1024);
          bool bad = false;
#pragma unroll
          for (int k = 0; k < 6; ++k) bad |= has_sent(p[k]);
          if (__builtin_amdgcn_ballot_w64(bad) == 0ull) break;
        }
        STAMP(8);
        f32x4 sa = __builtin_bit_cast(f32x4, p[0]);
#pragma unroll
        for (int k = 1; k < 6; ++k) sa += __builtin_bit_cast(f32x4, p[k]);
        float u = tr16(tr32(sa.x, sa.z), tr32(sa.y, sa.w));
        u += dpp<ROR8>(u);
        const float o = hv + rbf(rbf(u) + biasf);
        const rsrc_t ro = mkrs(out, pass ? NB * DM * 2 : 0);
        __builtin_amdgcn_raw_buffer_store_b16((u16)pk2(o, o), ro,
            (lane & 8) == 0 ? (((lane >> 2) & 1) * DM + 16 * mrole + 4 * (lane & 3) + (lane >> 5) * 2 + (r & 1)) * 2 : OOB, 0, 0);
        __builtin_amdgcn_raw_buffer_store_b32(tagA, rt, (mrole == 0 && lane == 0) ? OFF_EPOCH : OOB, 0, AUX_SC1);
        STAMP(9);
      }
    }
    if (pass) break;
    pass = 1;
    asm volatile("" : "+s"(pass));
    __builtin_amdgcn_s_waitcnt(0x0F70);
    STAMP(11);
  }
  asm volatile(".Lfb_end:");
  STAMP(10);
  pace(tprobe + 6000);
}

extern "C" void kernel_launch(void* const* d_in, const int* in_sizes, int n_in,
                              void* d_out, int out_size, void* d_ws, size_t ws_size,
                              hipStream_t stream) {
  fused_block<<<NWG, NTHR, 0, stream>>>((const u16*)d_in[0], (const u16*)d_in[1], (const u16*)d_in[2],
                                        (const u16*)d_in[3], (const u16*)d_in[4], (const u16*)d_in[6],
                                        (const u16*)d_in[7], (const u16*)d_in[8], (const u16*)d_in[9],
                                        (u16*)d_out, (unsigned char*)d_ws);
}
```

```cpp
#include <hip/hip_runtime.h>
#include <stdint.h>

typedef unsigned short u16;
typedef unsigned v4u __attribute__((ext_vector_type(4)));
typedef unsigned v2u __attribute__((ext_vector_type(2)));
typedef short v4s __attribute__((ext_vector_type(4)));
typedef __bf16 bf16x2 __attribute__((ext_vector_type(2)));
typedef __bf16 bf16x8 __attribute__((ext_vector_type(8)));
typedef float f32x4 __attribute__((ext_vector_type(4)));
typedef float f32x2 __attribute__((ext_vector_type(2)));
typedef __amdgpu_buffer_rsrc_t rsrc_t;
#define GAS __attribute__((address_space(1)))
#define LAS __attribute__((address_space(3)))

#define NQ0 3
#define NQ2 30
#define DIST 6
#define ENDV 5
#define TAU4 128
#define LEAD 2
#define W_AUX 2
#define DRY 1
#define REPA 8
#define D1 0
#define D2 12
#define D3 0
#define CPF 1
#define PFLY 4
#define RT1 2
#define FWAIT 800
#define TAU4B TAU4
#define NSW 32
#define WOP0 63
#define WOP1 63
#define W1_AUX 2
#define WQB 4
#define RP0 36
#define RPS 3
#define QMODE 1
#define QWAIT 550

constexpr int NB = 2, DM = 512, NH = 32, SK = 4096, DH = 128, FF = 1376;
constexpr int NWG = 256, NTHR = 576;
constexpr int OFF_EPOCH = 0;
constexpr int OFF_FLAG = 1024;
constexpr int OFF_LSE = 4096;
constexpr int OFF_OP = 65536;
constexpr int OFF_QX = 131072;
constexpr int OFF_PO = OFF_QX + NH * 2 * 8 * 1024;
constexpr int PO_CG = 43 * 128;
constexpr int OFF_AP = OFF_PO + 32 * PO_CG;
constexpr int WS_BYTES = OFF_AP + 8 * 16384;
constexpr int OFF_STAMP = 1 << 20;
constexpr int AUX_SC1 = 16;
constexpr int OOB = 0x7ffffff0;
constexpr unsigned SENT = 0xFFFFFFFFu;
constexpr unsigned SPIN_CAP = 1u << 16;
constexpr int M1_BASE = 0, M1_N = 43;
constexpr int M2_BASE = 64, M2_N = 32;
constexpr int L_Q = 0;
constexpr int L_OP = 512;
constexpr int L_P1 = 5120;
constexpr int L_P2 = 7424;
constexpr int L_ST = 12032;
constexpr int L_WO = 12288;
constexpr int L_XN = 35328;
constexpr int L_WQ = 128000;
constexpr int L_GX = 52736;
constexpr int L_TR = 54272;
constexpr int TR_W = 9216, TR_W2 = 4608;
constexpr int LDS_BYTES = L_WQ + 512 * 40;

template <int CTRL> __device__ __forceinline__ float dpp(float x) {
  return __builtin_bit_cast(float, __builtin_amdgcn_mov_dpp(__builtin_bit_cast(int, x), CTRL, 0xf, 0xf, true));
}
constexpr int XOR1 = 0xB1, XOR2 = 0x4E, XOR7 = 0x141, ROR1 = 0x121, ROR2 = 0x122, ROR4 = 0x124, ROR8 = 0x128, BCAST = 0x150;
__device__ __forceinline__ float lo16(unsigned v) { return __uint_as_float(v << 16); }
__device__ __forceinline__ float hi16(unsigned v) { return __uint_as_float(v & 0xffff0000u); }
__device__ __forceinline__ unsigned pk2(float a, float b) {
  f32x2 p = {a, b};
  return __builtin_bit_cast(unsigned, __builtin_convertvector(p, bf16x2));
}
__device__ __forceinline__ float rbf(float a) { return lo16(pk2(a, a)); }
__device__ __forceinline__ float dot2(unsigned a, unsigned b, float c) {
  return __builtin_amdgcn_fdot2_f32_bf16(__builtin_bit_cast(bf16x2, a), __builtin_bit_cast(bf16x2, b), c, false);
}
__device__ __forceinline__ f32x4 mfma(v4u a, v4u b, f32x4 c) {
  return __builtin_amdgcn_mfma_f32_16x16x32_bf16(__builtin_bit_cast(bf16x8, a), __builtin_bit_cast(bf16x8, b), c, 0, 0, 0);
}
__device__ __forceinline__ float tr32(float a, float b) {
  auto s = __builtin_amdgcn_permlane32_swap(__float_as_uint(a), __float_as_uint(b), false, false);
  return __uint_as_float(s[0]) + __uint_as_float(s[1]);
}
__device__ __forceinline__ float tr16(float a, float b) {
  auto s = __builtin_amdgcn_permlane16_swap(__float_as_uint(a), __float_as_uint(b), false, false);
  return __uint_as_float(s[0]) + __uint_as_float(s[1]);
}
__device__ __forceinline__ float wave_sum(float x) {
  x += dpp<ROR8>(x); x += dpp<ROR4>(x); x += dpp<ROR2>(x); x += dpp<ROR1>(x);
  x = tr16(x, x); x = tr32(x, x);
  return x;
}
__device__ __forceinline__ float wave_max(float x) {
  x = fmaxf(x, dpp<ROR8>(x)); x = fmaxf(x, dpp<ROR4>(x)); x = fmaxf(x, dpp<ROR2>(x)); x = fmaxf(x, dpp<ROR1>(x));
  auto s = __builtin_amdgcn_permlane16_swap(__float_as_uint(x), __float_as_uint(x), false, false);
  x = fmaxf(__uint_as_float(s[0]), __uint_as_float(s[1]));
  auto t = __builtin_amdgcn_permlane32_swap(__float_as_uint(x), __float_as_uint(x), false, false);
  return fmaxf(__uint_as_float(t[0]), __uint_as_float(t[1]));
}
template <int CTRL> __device__ __forceinline__ float trstep(float lo, float hi, bool bit) {
  const float keep = bit ? hi : lo, send = bit ? lo : hi;
  return keep + dpp<CTRL>(send);
}
__device__ __forceinline__ rsrc_t mkrs(const void* p, int bytes) {
  return __builtin_amdgcn_make_buffer_rsrc((void*)p, 0, bytes, 0x00020000);
}
template <int AUX> __device__ __forceinline__ v4u ld128(rsrc_t rs, int voff, int soff) {
  return __builtin_amdgcn_raw_buffer_load_b128(rs, voff, soff, AUX);
}
__device__ __forceinline__ void lds_barrier() {
  asm volatile("s_waitcnt lgkmcnt(0)" ::: "memory");
  __builtin_amdgcn_s_barrier();
  asm volatile("" ::: "memory");
}
__device__ __forceinline__ void lds_barrier_if(int on) {
  asm volatile("s_waitcnt lgkmcnt(0)\n\ts_cmp_eq_u32 %0, 0\n\ts_cbranch_scc1 1f\n\ts_barrier\n1:" :: "s"(on) : "scc", "memory");
}
__device__ __forceinline__ bool has_sent(v4u v) { return (v.x == SENT) | (v.y == SENT) | (v.z == SENT) | (v.w == SENT); }
__device__ __forceinline__ unsigned nosent(unsigned v) { return v < SENT ? v : SENT - 1u; }
constexpr int brev4(int i) { return ((i & 1) << 3) | ((i & 2) << 1) | ((i & 4) >> 1) | ((i & 8) >> 3); }
__device__ __forceinline__ void pace(unsigned target) {
  while ((int)((unsigned)__builtin_amdgcn_s_memrealtime() - target) < 0) __builtin_amdgcn_s_sleep(1);
}
__device__ __forceinline__ v4u trfrag(LAS unsigned char* p, int rs4) {
  const v2u u0 = __builtin_bit_cast(v2u, __builtin_amdgcn_ds_read_tr16_b64_v4i16((LAS v4s*)p));
  const v2u u1 = __builtin_bit_cast(v2u, __builtin_amdgcn_ds_read_tr16_b64_v4i16((LAS v4s*)(p + rs4)));
  return v4u{u0.x, u0.y, u1.x, u1.y};
}
__device__ __forceinline__ void repoll128(v4u& a, rsrc_t rs, int off) {
  _Pragma("nounroll") for (unsigned spins = 0; spins < SPIN_CAP; ++spins) {
    const bool bad = has_sent(a);
    if (__builtin_amdgcn_ballot_w64(bad) == 0ull) break;
    asm volatile("" : "+v"(off) :: "memory");
    if (bad) a = ld128<AUX_SC1>(rs, off, 0);
  }
}
__device__ __forceinline__ float half_sum(float x) {
  x += dpp<ROR8>(x); x += dpp<ROR4>(x); x += dpp<ROR2>(x); x += dpp<ROR1>(x);
  return tr16(x, x);
}

template <int I> __device__ __forceinline__ void axpy8(float (&acc)[8], float pv, v4u v) {
  const float p = dpp<BCAST + I>(pv);
#pragma unroll
  for (int k = 0; k < 4; ++k) {
    acc[2 * k] = fmaf(p, lo16(v[k]), acc[2 * k]);
    acc[2 * k + 1] = fmaf(p, hi16(v[k]), acc[2 * k + 1]);
  }
}
__device__ __forceinline__ void pin8(float (&a)[8]) {
  asm volatile("" : "+v"(a[0]), "+v"(a[1]), "+v"(a[2]), "+v"(a[3]), "+v"(a[4]), "+v"(a[5]), "+v"(a[6]), "+v"(a[7]));
}
__device__ __forceinline__ void pin1(float& a) { asm volatile("" : "+v"(a)); }
constexpr int kissued(int i) { return (NQ2 + (i + 3) / 4 > i + DIST) ? NQ2 + (i + 3) / 4 : i + DIST; }
static_assert(RP0 + DIST + 7 * RPS <= 63, "the last MLP weight load is stored inside the stream");
static_assert(kissued(32) == 32 + DIST, "the distance is reached inside the K phase");
template <int I, int N, typename F> __device__ __forceinline__ void sfor(F&& f) {
  if constexpr (I < N) { f(std::integral_constant<int, I>{}); sfor<I + 1, N>(f); }
}

#define STAMP(i) do {} while (0)

extern "C" __global__ void __launch_bounds__(NTHR)
fused_block(const u16* __restrict__ x, const u16* __restrict__ kc, const u16* __restrict__ vc,
            const u16* __restrict__ wq, const u16* __restrict__ wo, const u16* __restrict__ w1,
            const u16* __restrict__ b1, const u16* __restrict__ w2, const u16* __restrict__ b2,
            u16* out, unsigned char* ws) {
  __shared__ __attribute__((aligned(16))) unsigned char smem[LDS_BYTES];
  LAS unsigned char* L = (LAS unsigned char*)smem;
  const unsigned t0 = (unsigned)__builtin_amdgcn_s_memrealtime();
  const int tid = threadIdx.x, lane = tid & 63, w = (__builtin_amdgcn_readfirstlane(tid >> 6) + 8) % 9;
  const int r = lane >> 4, c = lane & 15;
  const int bid = blockIdx.x;
  const int kr = bid & 7, cg = bid >> 3;
  const int h = kr * 4 + (cg & 3), b = bid >> 7, s = (bid >> 5) & 3;
  const int bh = b * NH + h;
  const int l16 = lane * 16;
  const rsrc_t rs = mkrs(ws, WS_BYTES);
  const bool is_m1 = cg < 6 && kr * 6 + cg < M1_N;
  const bool is_m2 = bid >= M2_BASE && bid < M2_BASE + M2_N;
  const int mrole = is_m1 ? kr * 6 + cg : is_m2 ? bid - M2_BASE : 0;
  constexpr float LOG2E = 1.4426950408889634f;
  const f32x4 z4 = {0.f, 0.f, 0.f, 0.f};
  const v4u zero4 = {0u, 0u, 0u, 0u};
  const v4u ones = {0x3F803F80u, 0x3F803F80u, 0x3F803F80u, 0x3F803F80u};
  v4u ve[ENDV], gl = zero4, wov[2] = {zero4, zero4};
#pragma unroll
  for (int i = 0; i < ENDV; ++i) ve[i] = zero4;
  float acc[8], pr1 = 0.f, mw = 0.f, biasf = 0.f;
#pragma unroll
  for (int k = 0; k < 8; ++k) acc[k] = 0.f;
  unsigned tagA, tagB, xo = 0u;
  const int lse_off = OFF_LSE + (bh * 32 + (lane & 31)) * 16;
  const int ap_rep = (c & 3) + 4 * (r >> 1);
  const int ap_st = ap_rep < REPA ? OFF_AP + ap_rep * 16384 + ((cg >> 1) * 16 + kr * 2 + (r & 1)) * 64 + (cg & 1) * 32 + (c >> 2) * 8 : OOB;

  const int tw = (lane >> 1) * 40 + (lane & 1) * 16, tra = (8 * r + (c >> 2)) * 40 + (c & 3) * 8;
  if (w < 8) {
    v4u kv[64];
    const size_t kvoff = ((size_t)bh * SK + s * 1024 + w * 128) * DH;
    const rsrc_t rk = mkrs(kc + kvoff, 32768), rv = mkrs(vc + kvoff, 32768);
    const rsrc_t ro = mkrs(wo + (size_t)(kr * 512 + w * 64) * DM + cg * 16, 64 * DM * 2 - cg * 32);
    const rsrc_t r1 = mkrs(w1 + (size_t)(64 * w) * FF + 32 * mrole, is_m1 ? 64 * FF * 2 - 64 * mrole : 0);
    const rsrc_t r2 = mkrs(w2 + 32 * mrole * DM + 64 * w, is_m1 ? 32 * DM * 2 - 128 * w : 0);
    v4u tv[8];
    auto issue = [&](auto N) {
      constexpr int n = N.value;
      if constexpr (n < 64) {
        if constexpr (TAU4 > 0 && n >= NQ0 && ((n - NQ0) & 1) == 0) pace(t0 + (unsigned)(n <= NSW ? (n - LEAD) * TAU4 / 4 : ((NSW - LEAD) * TAU4 + (n - NSW) * TAU4B) / 4));
        if constexpr (n >= PFLY && n < kissued(32 - DIST)) asm volatile("s_waitcnt vmcnt(%0)" :: "i"(PFLY - 1));
        if constexpr (n < 32) kv[n] = ld128<2>(rk, l16, n * 1024); else kv[n] = ld128<2>(rv, l16, (n - 32) * 1024);
        if constexpr (n >= RP0 && (n - RP0) % RPS == 0 && (n - RP0) / RPS < 8) {
          constexpr int j = (n - RP0) / RPS;
          if constexpr (j & 1) tv[j] = ld128<W_AUX>(r2, (lane >> 3) * (DM * 2) + (lane & 7) * 16, (j >> 1) * 8 * DM * 2);
          else tv[j] = ld128<W1_AUX>(r1, (lane >> 2) * (FF * 2) + (lane & 3) * 16, (j >> 1) * 16 * FF * 2);
        }
        if constexpr (n >= RP0 + DIST && (n - RP0 - DIST) % RPS == 0 && (n - RP0 - DIST) / RPS < 8) {
          constexpr int j = (n - RP0 - DIST) / RPS;
          LAS unsigned char* T = L + L_TR + w * TR_W;
          if constexpr (j & 1) *(LAS v4u*)(T + TR_W2 + ((j >> 1) * 8 + (lane >> 3)) * 144 + (lane & 7) * 16) = tv[j];
          else {
            LAS unsigned char* d = T + ((j >> 1) * 16 + (lane >> 2)) * 72 + (lane & 3) * 16;
            *(LAS v2u*)d = v2u{tv[j].x, tv[j].y}; *(LAS v2u*)(d + 8) = v2u{tv[j].z, tv[j].w};
          }
        }
        if constexpr (n == WOP0) wov[0] = ld128<W_AUX>(ro, (lane >> 1) * (DM * 2) + (lane & 1) * 16, 0);
        if constexpr (n == WOP1) wov[1] = ld128<W_AUX>(ro, (lane >> 1) * (DM * 2) + (lane & 1) * 16, 32 * DM * 2);
        __builtin_amdgcn_sched_barrier(0);
      }
    };
    sfor<0, NQ0>(issue);
    __builtin_amdgcn_s_barrier();
    asm volatile("" ::: "memory");
    sfor<NQ0, NQ2>(issue);
    _Pragma("nounroll") for (unsigned spins = 0; spins < (SPIN_CAP << 6); ++spins) {
      if (__builtin_amdgcn_readfirstlane(*(volatile LAS unsigned*)(L + L_Q + 264)) != 0u) break;
      __builtin_amdgcn_s_sleep(1);
    }
    asm volatile("" ::: "memory");
    const v4u q = *(LAS v4u*)(L + L_Q + c * 16);
    {
      const v2u tg = *(LAS v2u*)(L + L_Q + 256);
      tagA = __builtin_amdgcn_readfirstlane(tg.x); tagB = __builtin_amdgcn_readfirstlane(tg.y);
    }
    STAMP(1);
    __builtin_amdgcn_sched_barrier(0);
    float sc[2];
    {
      const bool b3 = c & 8, b2_ = c & 4, b1_ = c & 2, b0 = c & 1;
      float la, lb, lc, ld;
      sfor<0, 32>([&](auto I) {
        constexpr int i = I.value;
        const v4u kk = kv[i];
        float d = dot2(kk.x, q.x, 0.f);
        d = dot2(kk.y, q.y, d); d = dot2(kk.z, q.z, d); d = dot2(kk.w, q.w, d);
        if constexpr ((i & 1) != 0) {
          d = trstep<ROR8>(la, d, b3);
          if constexpr ((i & 2) != 0) {
            d = trstep<XOR7>(lb, d, b2_);
            if constexpr ((i & 4) != 0) {
              d = trstep<XOR2>(lc, d, b1_);
              if constexpr ((i & 8) != 0) d = trstep<XOR1>(ld, d, b0);
            }
          }
        }
        pin1(d);
        if constexpr ((i & 1) == 0) la = d; else if constexpr ((i & 2) == 0) lb = d; else if constexpr ((i & 4) == 0) lc = d;
        else if constexpr ((i & 8) == 0) ld = d; else sc[i >> 4] = d;
        __builtin_amdgcn_sched_barrier(0);
        sfor<kissued(i), kissued(i + 1)>(issue);
      });
    }
    STAMP(2);
    float pr[2];
    {
      const float s0 = rbf(rbf(sc[0]) * 0.08838834764831845f), s1 = rbf(rbf(sc[1]) * 0.08838834764831845f);
      mw = wave_max(fmaxf(s0, s1));
      pr[0] = __builtin_amdgcn_exp2f((s0 - mw) * LOG2E);
      pr[1] = __builtin_amdgcn_exp2f((s1 - mw) * LOG2E);
      const float lw = wave_sum(pr[0] + pr[1]);
      __builtin_amdgcn_raw_buffer_store_b128(v4u{__float_as_uint(mw), tagA, __float_as_uint(lw), tagB}, rs,
                                             lane == 0 ? OFF_LSE + ((bh * 4 + s) * 8 + w) * 16 : OOB, 0, AUX_SC1);
    }
    __builtin_amdgcn_sched_barrier(0);
    sfor<0, 32 - DIST>([&](auto J) {
      constexpr int j = J.value;
      constexpr int c0 = (j * (32 - ENDV) + 31 - DIST) / (32 - DIST), c1 = ((j + 1) * (32 - ENDV) + 31 - DIST) / (32 - DIST);
      sfor<c0, c1>([&](auto I) {
        constexpr int i = I.value;
        axpy8<brev4(i & 15)>(acc, pr[i >> 4], kv[32 + i]);
        pin8(acc);
        __builtin_amdgcn_sched_barrier(0);
      });
      issue(std::integral_constant<int, 32 + j + DIST>{});
      if constexpr (j == 32 - DIST - ENDV - 1) {
        gl = ld128<AUX_SC1>(rs, lse_off, 0);
        __builtin_amdgcn_sched_barrier(0);
      }
    });
#pragma unroll
    for (int i = 0; i < ENDV; ++i) ve[i] = kv[64 - ENDV + i];
    pr1 = pr[1];
  } else {
    __builtin_amdgcn_s_setprio(3);
    v4u cpf = zero4;
    if (CPF) {
      unsigned long long pc0; unsigned back, len;
      asm volatile("s_getpc_b64 %0\n.Lfb_here:\n\ts_mov_b32 %1, .Lfb_here - fused_block\n\ts_mov_b32 %2, .Lfb_end - fused_block"
                   : "=s"(pc0), "=s"(back), "=s"(len));
      pc0 -= back;
      cpf = ld128<0>(mkrs((const void*)pc0, (int)len), cg * 1024 + l16, 0);
    }
    const rsrc_t rx = mkrs(x, NB * DM * 2);
    const v2u e = __builtin_amdgcn_raw_buffer_load_b64(rs, OFF_EPOCH, 0, 2);
    const v4u xs0 = ld128<2>(rx, lane * 32, 0), xs1 = ld128<2>(rx, lane * 32 + 16, 0);
    v4u wr[16];
    const int xbo = ((c & 1) * DM + 64 * kr + 8 * r) * 2;
    const v4u xb0 = ld128<2>(rx, xbo, 0), xb1 = ld128<2>(rx, xbo + 64, 0);
    {
      const rsrc_t rq = mkrs(wq + (size_t)(64 * kr) * (NH * DH) + cg * DH, 64 * NH * DH * 2 - cg * DH * 2);
#pragma unroll
      for (int j = 0; j < 16; ++j) wr[j] = ld128<2>(rq, r * (NH * DH * 2) + c * 16, j * 4 * NH * DH * 2);
    }
    *(LAS unsigned*)(L + L_Q + 264) = 0u;
    lds_barrier();
    STAMP(13);
    {
      __builtin_amdgcn_raw_buffer_store_b32(SENT, rs, OFF_OP + (h * 8 + b * 4 + s) * 256 + lane * 4, 0, AUX_SC1);
      __builtin_amdgcn_raw_buffer_store_b64(v2u{SENT, SENT}, rs, ap_st, 0, AUX_SC1);
      const int co = is_m1 ? OFF_PO + (lane >> 3) * PO_CG + mrole * 128 + (lane & 7) * 16 : OOB;
#pragma unroll
      for (int k = 0; k < 4; ++k) __builtin_amdgcn_raw_buffer_store_b128(v4u{SENT, SENT, SENT, SENT}, rs, co, k * 8 * PO_CG, AUX_SC1);
    }
    __builtin_amdgcn_sched_barrier(0);
    v4u xn0, xn1;
    {
      float sm = 0.f;
#pragma unroll
      for (int k = 0; k < 4; ++k) { sm = dot2(xs0[k], 0x3F803F80u, sm); sm = dot2(xs1[k], 0x3F803F80u, sm); }
      const float mean = half_sum(sm) * (1.f / DM);
      float sq = 0.f;
#pragma unroll
      for (int k = 0; k < 4; ++k) {
        const float d0 = lo16(xs0[k]) - mean, d1 = hi16(xs0[k]) - mean, d2 = lo16(xs1[k]) - mean, d3 = hi16(xs1[k]) - mean;
        sq = fmaf(d0, d0, sq); sq = fmaf(d1, d1, sq); sq = fmaf(d2, d2, sq); sq = fmaf(d3, d3, sq);
      }
      const float var = half_sum(sq) * (1.f / DM);
      const float mb = rbf(mean), rd = 1.f / rbf(__builtin_amdgcn_sqrtf(rbf(rbf(var) + 1e-5f)));
      const float m0 = __uint_as_float(__builtin_amdgcn_readlane(__float_as_uint(mb), 0)), m1 = __uint_as_float(__builtin_amdgcn_readlane(__float_as_uint(mb), 32));
      const float r0 = __uint_as_float(__builtin_amdgcn_readlane(__float_as_uint(rd), 0)), r1 = __uint_as_float(__builtin_amdgcn_readlane(__float_as_uint(rd), 32));
      const float mm = (c & 1) ? m1 : m0, rr = (c & 1) ? r1 : r0;
#pragma unroll
      for (int k = 0; k < 4; ++k) {
        const unsigned a0 = pk2(lo16(xb0[k]) - mm, hi16(xb0[k]) - mm), a1 = pk2(lo16(xb1[k]) - mm, hi16(xb1[k]) - mm);
        xn0[k] = pk2(lo16(a0) * rr, hi16(a0) * rr);
        xn1[k] = pk2(lo16(a1) * rr, hi16(a1) * rr);
      }
      const unsigned e0 = __builtin_amdgcn_readfirstlane(e.x), e1 = __builtin_amdgcn_readfirstlane(e.y);
      tagA = e0 + 1u;
      tagB = (e1 ^ (e0 * 0x9E3779B1u)) + 0x7F4A7C15u;
    }
    asm volatile("" : "+v"(xn0), "+v"(xn1) : "v"(cpf));
    STAMP(14);
    {
#pragma unroll
      for (int j = 0; j < 16; ++j) *(LAS v4u*)(L + L_WQ + (4 * j + r) * 272 + c * 16) = wr[j];
      const int ra = L_WQ + (8 * r + (c >> 2)) * 272 + (c & 3) * 8;
      f32x4 d = z4;
#pragma unroll
      for (int n = 0; n < 8; ++n) {
        const f32x4 dn = mfma(trfrag(L + ra + 32 * 272 + n * 32, 4 * 272), xn1, mfma(trfrag(L + ra + n * 32, 4 * 272), xn0, z4));
        if ((c >> 1) == n) d = dn;
      }
      const int qo = OFF_QX + (((cg * 2 + (c & 1)) * 8 + kr) * 64 + (c >> 1) * 8 + 2 * r) * 16;
      __builtin_amdgcn_raw_buffer_store_b128(v4u{__float_as_uint(d.x), tagA, __float_as_uint(d.y), tagB}, rs, qo, 0, AUX_SC1);
      __builtin_amdgcn_raw_buffer_store_b128(v4u{__float_as_uint(d.z), tagA, __float_as_uint(d.w), tagB}, rs, qo + 16, 0, AUX_SC1);
      STAMP(12);
      asm volatile("s_waitcnt vmcnt(0)" ::: "memory");
      __builtin_amdgcn_raw_buffer_store_b64(v2u{tagA, tagB}, rs, lane == 0 ? OFF_FLAG + (kr * 32 + cg) * 8 : OOB, 0, AUX_SC1);
    }
    {
      const int qx_off = OFF_QX + (h * 2 + b) * 8192 + l16;
      v4u qx[8];
      pace(t0 + QWAIT);
      _Pragma("nounroll") for (unsigned spins = 0; spins < SPIN_CAP; ++spins) {
        asm volatile("" ::: "memory");
        bool bad = false;
#pragma unroll
        for (int k = 0; k < 8; ++k) qx[k] = ld128<AUX_SC1>(rs, qx_off, k * 1024);
#pragma unroll
        for (int k = 0; k < 8; ++k) bad |= (qx[k].y != tagA) | (qx[k].w != tagB);
        if (__builtin_amdgcn_ballot_w64(bad) == 0ull) break;
        __builtin_amdgcn_s_sleep(16);
      }
      float q0 = 0.f, q1 = 0.f;
#pragma unroll
      for (int k = 0; k < 8; ++k) { q0 += __uint_as_float(qx[k].x); q1 += __uint_as_float(qx[k].z); }
      *(LAS unsigned*)(L + L_Q + lane * 4) = pk2(q0, q1);
      *(LAS v2u*)(L + L_Q + 256) = v2u{tagA, tagB};
      asm volatile("s_waitcnt lgkmcnt(0)" ::: "memory");
      *(volatile LAS unsigned*)(L + L_Q + 264) = 1u;
    }
    STAMP(15);
    if (is_m1) {
      *(LAS float*)(L + L_Q + 272 + (lane & 31) * 4) = lo16(__builtin_amdgcn_raw_buffer_load_b16(mkrs(b1 + 32 * mrole, 64), (lane & 31) * 2, 0, 0));
    }
    if (is_m2) biasf = lo16(__builtin_amdgcn_raw_buffer_load_b16(mkrs(b2 + 16 * mrole, 32), ((lane & 3) * 4 + (lane >> 5) * 2 + (r & 1)) * 2, 0, 0));
    xo = __builtin_amdgcn_raw_buffer_load_b32(rx, kr == 0 ? ((r & 1) * DM + cg * 16 + (c & 14)) * 2 : OOB, 0, 0);
    {
      const int fo0 = OFF_FLAG + ((is_m1 || is_m2) ? l16 : kr * 256 + c * 16), fo1 = (is_m1 || is_m2) ? fo0 + 1024 : fo0;
      pace(t0 + FWAIT + cg * 8);
      _Pragma("nounroll") for (unsigned spins = 0; spins < SPIN_CAP; ++spins) {
        asm volatile("" ::: "memory");
        const v4u fl0 = ld128<AUX_SC1>(rs, fo0, 0), fl1 = ld128<AUX_SC1>(rs, fo1, 0);
        if (__builtin_amdgcn_ballot_w64(fl0.x != tagA || fl0.y != tagB || fl0.z != tagA || fl0.w != tagB ||
                                        fl1.x != tagA || fl1.y != tagB || fl1.z != tagA || fl1.w != tagB) == 0ull) break;
        __builtin_amdgcn_s_sleep(32);
      }
    }
    asm volatile("" : "+v"(xo), "+v"(biasf));
    STAMP(2);
  }

  int pass = DRY ? (w ^ 8) : 1;
  asm volatile("" : "+s"(pass));
#pragma nounroll
  for (;;) {
    const bool doA = w < 8 || !pass, doB = w == 8;
    const rsrc_t rt = mkrs(ws, pass ? WS_BYTES : 0);
    if (doA) {
      if (pass && __builtin_amdgcn_ballot_w64(gl.y != tagA || gl.w != tagB) != 0ull) {
        _Pragma("nounroll") for (unsigned spins = 0; spins < SPIN_CAP; ++spins) {
          asm volatile("" ::: "memory");
          gl = ld128<AUX_SC1>(rs, lse_off, 0);
          if (__builtin_amdgcn_ballot_w64(gl.y != tagA || gl.w != tagB) == 0ull) break;
        }
      }
      const float m_i = __uint_as_float(gl.x), l_i = __uint_as_float(gl.z);
      const float M = wave_max(m_i);
      const float Ls = wave_sum(l_i * __builtin_amdgcn_exp2f((m_i - M) * LOG2E)) * 0.5f;
      float alpha = __builtin_amdgcn_exp2f((mw - M) * LOG2E) / Ls;
      pin1(alpha);
      __builtin_amdgcn_sched_barrier(0);
      sfor<0, ENDV>([&](auto I) {
        constexpr int i = I.value;
        asm volatile("" : "+v"(ve[i]));
        axpy8<brev4((32 - ENDV + i) & 15)>(acc, pr1, ve[i]);
        pin8(acc);
        __builtin_amdgcn_sched_barrier(0);
      });
      STAMP(3);
      const float t0_ = tr32(acc[0], acc[4]), t1 = tr32(acc[1], acc[5]), t2 = tr32(acc[2], acc[6]), t3 = tr32(acc[3], acc[7]);
      f32x2 o2 = {tr16(t0_, t2) * alpha, tr16(t1, t3) * alpha};
      *(LAS f32x2*)(L + L_OP + (w * DH + 8 * c + 2 * r) * 4) = o2;
    }
    lds_barrier_if(pass);
    if (doB) {
      f32x2 o2 = {0.f, 0.f};
#pragma unroll
      for (int ww = 0; ww < 8; ++ww) o2 += *(LAS f32x2*)(L + L_OP + (ww * DH + 2 * lane) * 4);
      __builtin_amdgcn_raw_buffer_store_b32(nosent(pk2(o2.x, o2.y)), rt, OFF_OP + (h * 8 + b * 4 + s) * 256 + lane * 4, 0, AUX_SC1);
      STAMP(4);
    }
    v4u bf[8];
#pragma unroll
    for (int j = 0; j < 8; ++j) bf[j] = zero4;
    float cs = 0.f;
    if (doA) {
      if (is_m1) {
        LAS unsigned char* T = L + L_TR + (w & 7) * TR_W;
#pragma unroll
        for (int j = 0; j < 4; ++j) {
          bf[j] = trfrag(T + (8 * r + (c >> 2)) * 72 + (c & 3) * 8 + (j >> 1) * 32 * 72 + (j & 1) * 32, 288);
          bf[4 + j] = trfrag(T + TR_W2 + (8 * r + (c >> 2)) * 144 + (c & 3) * 8 + j * 32, 576);
        }
        const f32x4 c0 = mfma(ones, bf[2], mfma(ones, bf[0], z4)), c1 = mfma(ones, bf[3], mfma(ones, bf[1], z4));
        cs = r < 2 ? c0.x : c1.x;
        biasf = *(LAS float*)(L + L_Q + 272 + ((r >> 1) * 16 + c) * 4);
        pin1(cs); pin1(biasf);
      }
      __builtin_amdgcn_sched_barrier(0);
      const int po = OFF_OP + (((kr * 4 + ((w & 7) >> 1)) * 8 + (c & 7)) * DH + 64 * (w & 1) + 32 * (c >> 3) + 8 * r) * 2;
      if (D1) __builtin_amdgcn_s_sleep(D1);
      v4u a = ld128<AUX_SC1>(rt, po, 0);
      __builtin_amdgcn_sched_barrier(0);
      LAS unsigned char* T = L + L_WO + w * 2560;
      v4u bo[2];
#pragma unroll
      for (int j = 0; j < 2; ++j) {
        *(LAS v2u*)(T + tw + j * 1280) = v2u{wov[j].x, wov[j].y};
        *(LAS v2u*)(T + tw + j * 1280 + 8) = v2u{wov[j].z, wov[j].w};
        bo[j] = trfrag(T + tra + j * 1280, 160);
      }
      repoll128(a, rt, po);
      STAMP(5);
      const f32x4 d0 = mfma(a, bo[0], z4), d1 = mfma(a, bo[1], z4);
      const f32x4 d = r < 2 ? d0 : d1;
      *(LAS float*)(L + L_P1 + (w * 64 + lane) * 4) = (d.x + d.y) + (d.z + d.w);
    }
    lds_barrier_if(pass);
    if (doB) {
      float u = 0.f;
#pragma unroll
      for (int ww = 0; ww < 8; ++ww) u += *(LAS float*)(L + L_P1 + (ww * 64 + lane) * 4);
      u = tr32(u, u) + ((c & 1) ? hi16(xo) : lo16(xo));
      const unsigned p01 = nosent(pk2(dpp<0x00>(u), dpp<0x55>(u))), p23 = nosent(pk2(dpp<0xAA>(u), dpp<0xFF>(u)));
      __builtin_amdgcn_raw_buffer_store_b64(v2u{p01, p23}, rt, ap_st, 0, AUX_SC1);
      STAMP(6);
    }
    if (is_m1) {
      if (doA) {
        const int ao = OFF_AP + (kr % REPA) * 16384 + (w & 7) * 2048 + c * 64 + r * 16;
        if (D2) __builtin_amdgcn_s_sleep(D2);
        v4u a0 = ld128<AUX_SC1>(rt, ao, 0), a1 = ld128<AUX_SC1>(rt, ao + 1024, 0);
        _Pragma("nounroll") for (unsigned spins = 0; spins < SPIN_CAP; ++spins) {
          const bool bad = has_sent(a0) || has_sent(a1);
          if (__builtin_amdgcn_ballot_w64(bad) == 0ull) break;
          asm volatile("" ::: "memory");
          if (bad) { a0 = ld128<AUX_SC1>(rt, ao, 0); a1 = ld128<AUX_SC1>(rt, ao + 1024, 0); }
        }
        STAMP(7);
        f32x4 d0 = mfma(a0, bf[0], z4), d1 = mfma(a0, bf[1], z4), dg = mfma(a0, a0, z4), dsum = mfma(a0, ones, z4);
        d0 = mfma(a1, bf[2], d0); d1 = mfma(a1, bf[3], d1); dg = mfma(a1, a1, dg); dsum = mfma(a1, ones, dsum);
        const float u = tr16(tr32(d0.x + d0.z, d1.x + d1.z), tr32(d0.y + d0.w, d1.y + d1.w));
        float gq = (c & 1) ? dg.y + dg.w : dg.x + dg.z;
        gq += dpp<ROR2>(gq); gq += dpp<ROR4>(gq); gq += dpp<ROR8>(gq);
        const float sh = (c & 1) ? dsum.y + dsum.w : dsum.x + dsum.z;
        float st = tr32(gq, sh);
        st = tr16(st, st);
        *(LAS f32x2*)(L + L_P2 + (w * 64 + lane) * 8) = f32x2{u, cs};
        if ((lane & 30) == 0) *(LAS float*)(L + L_ST + (w < 8 ? (((c & 1) * 8 + w) * 2 + (r >> 1)) * 4 : 128)) = st;
      }
      lds_barrier_if(pass);
      if (doA) {
        f32x2 uc = {0.f, 0.f};
#pragma unroll
        for (int ww = 0; ww < 8; ++ww) uc += *(LAS f32x2*)(L + L_P2 + (ww * 64 + lane) * 8);
        f32x4 sv = z4;
#pragma unroll
        for (int k = 0; k < 4; ++k) sv += *(LAS f32x4*)(L + L_ST + (r & 1) * 64 + k * 16);
        const float mean = (sv.y + sv.w) * (1.f / DM);
        const float var = fmaxf((sv.x + sv.z) * (1.f / DM) - mean * mean, 0.f);
        const float rd = __builtin_amdgcn_rcpf(rbf(__builtin_amdgcn_sqrtf(rbf(rbf(var) + 1e-5f))));
        const float z = rbf(rbf((uc.x - rbf(mean) * uc.y) * rd) + biasf);
        const float inner = 0.7978845608028654f * (z + 0.044715f * z * z * z);
        const float g = z * __builtin_amdgcn_rcpf(1.f + __builtin_amdgcn_exp2f(-2.f * LOG2E * inner));
        *(LAS u16*)(L + L_GX + w * 128 + (r & 1) * 64 + ((r >> 1) * 16 + c) * 2) = (u16)pk2(g, g);
        const v4u gb = *(LAS v4u*)(L + L_GX + w * 128 + (c & 1) * 64 + r * 16);
        const f32x4 d0 = mfma(bf[4], gb, z4), d1 = mfma(bf[5], gb, z4), d2 = mfma(bf[6], gb, z4), d3 = mfma(bf[7], gb, z4);
        const f32x4 d = (c & 4) ? ((c & 2) ? d3 : d2) : ((c & 2) ? d1 : d0);
        const v4u dv = {nosent(__float_as_uint(d.x)), nosent(__float_as_uint(d.y)), nosent(__float_as_uint(d.z)), nosent(__float_as_uint(d.w))};
        __builtin_amdgcn_raw_buffer_store_b128(dv, rt, c < 8 ? OFF_PO + ((w & 7) * 4 + (c >> 1)) * PO_CG + mrole * 128 + (c & 1) * 64 + r * 16 : OOB, 0, AUX_SC1);
        STAMP(8);
      }
    } else if (is_m2) {
      if (doB) {
        const int ho = OFF_AP + (kr % REPA) * 16384 + ((mrole >> 1) * 16 + (lane >> 2)) * 64 + (mrole & 1) * 32 + (lane & 3) * 8;
        if (D2) __builtin_amdgcn_s_sleep(D2);
        v2u hw;
        _Pragma("nounroll") for (unsigned spins = 0; spins < SPIN_CAP; ++spins) {
          asm volatile("" ::: "memory");
          hw = __builtin_amdgcn_raw_buffer_load_b64(rt, ho, 0, AUX_SC1);
          if (__builtin_amdgcn_ballot_w64(hw.x == SENT || hw.y == SENT) == 0ull) break;
        }
        float hv = tr16(tr32(lo16(hw.x), lo16(hw.y)), tr32(hi16(hw.x), hi16(hw.y)));
        hv = rbf(hv + dpp<ROR8>(hv));
        pin1(hv);
        STAMP(7);
        const int po = OFF_PO + mrole * PO_CG + l16, po5 = lane < 24 ? po : OOB;
        v4u p[6];
        if (D3) __builtin_amdgcn_s_sleep(D3);
        _Pragma("nounroll") for (unsigned spins = 0; spins < SPIN_CAP; ++spins) {
          asm volatile("" ::: "memory");
#pragma unroll
          for (int k = 0; k < 6; ++k) p[k] = ld128<AUX_SC1>(rt, k < 5 ? po : po5, k * 1024);
          bool bad = false;
#pragma unroll
          for (int k = 0; k < 6; ++k) bad |= has_sent(p[k]);
          if (__builtin_amdgcn_ballot_w64(bad) == 0ull) break;
        }
        STAMP(8);
        f32x4 sa = __builtin_bit_cast(f32x4, p[0]);
#pragma unroll
        for (int k = 1; k < 6; ++k) sa += __builtin_bit_cast(f32x4, p[k]);
        float u = tr16(tr32(sa.x, sa.z), tr32(sa.y, sa.w));
        u += dpp<ROR8>(u);
        const float o = hv + rbf(rbf(u) + biasf);
        const rsrc_t ro = mkrs(out, pass ? NB * DM * 2 : 0);
        __builtin_amdgcn_raw_buffer_store_b16((u16)pk2(o, o), ro,
            (lane & 8) == 0 ? (((lane >> 2) & 1) * DM + 16 * mrole + 4 * (lane & 3) + (lane >> 5) * 2 + (r & 1)) * 2 : OOB, 0, 0);
        __builtin_amdgcn_raw_buffer_store_b32(tagA, rt, (mrole == 0 && lane == 0) ? OFF_EPOCH : OOB, 0, AUX_SC1);
        STAMP(9);
      }
    }
    if (pass) break;
    pass = 1;
    asm volatile("" : "+s"(pass));
    __builtin_amdgcn_s_waitcnt(0x0F70);
    STAMP(11);
  }
  asm volatile(".Lfb_end:");
}

extern "C" void kernel_launch(void* const* d_in, const int* in_sizes, int n_in,
                              void* d_out, int out_size, void* d_ws, size_t ws_size,
                              hipStream_t stream) {
  fused_block<<<NWG, NTHR, 0, stream>>>((const u16*)d_in[0], (const u16*)d_in[1], (const u16*)d_in[2],
                                        (const u16*)d_in[3], (const u16*)d_in[4], (const u16*)d_in[6],
                                        (const u16*)d_in[7], (const u16*)d_in[8], (const u16*)d_in[9],
                                        (u16*)d_out, (unsigned char*)d_ws);
}
```

```cpp
#include <hip/hip_runtime.h>
#include <stdint.h>

typedef unsigned short u16;
typedef unsigned v4u __attribute__((ext_vector_type(4)));
typedef unsigned v2u __attribute__((ext_vector_type(2)));
typedef short v4s __attribute__((ext_vector_type(4)));
typedef __bf16 bf16x2 __attribute__((ext_vector_type(2)));
typedef __bf16 bf16x8 __attribute__((ext_vector_type(8)));
typedef float f32x4 __attribute__((ext_vector_type(4)));
typedef float f32x2 __attribute__((ext_vector_type(2)));
typedef __amdgpu_buffer_rsrc_t rsrc_t;
#define GAS __attribute__((address_space(1)))
#define LAS __attribute__((address_space(3)))

#define NQ0 6
#define NQ2 30
#define DIST 6
#define ENDV 5
#define TAU4 128
#define LEAD 2
#define W_AUX 2
#define DRY 1
#define REPA 2
#define D1 0
#define D2 12
#define D3 0
#define CPF 1
#define PFLY 6
#define RT1 2
#define FWAIT 800
#define TAU4B TAU4
#define NSW 32
#define WOP0 63
#define WOP1 63
#define W1_AUX 2
#define WQB 4
#define RP0 36
#define RPS 3
#define EVENROLE 1
#define WOBAR 1
#define QWAIT 550

constexpr int NB = 2, DM = 512, NH = 32, SK = 4096, DH = 128, FF = 1376;
constexpr int NWG = 256, NTHR = 576;
constexpr int OFF_EPOCH = 0;
constexpr int OFF_FLAG = 1024;
constexpr int OFF_LSE = 4096;
constexpr int OFF_OP = 65536;
constexpr int OFF_QX = 131072;
constexpr int OFF_PO = OFF_QX + NH * 2 * 8 * 1024;
constexpr int PO_CG = 43 * 128;
constexpr int OFF_AP = OFF_PO + 32 * PO_CG;
constexpr int WS_BYTES = OFF_AP + REPA * 16384;
constexpr int OFF_STAMP = 1 << 20;
constexpr int AUX_SC1 = 16;
constexpr int OOB = 0x7ffffff0;
constexpr unsigned SENT = 0xFFFFFFFFu;
constexpr unsigned SPIN_CAP = 1u << 16;
constexpr int M1_BASE = 0, M1_N = 43;
constexpr int M2_BASE = 128, M2_N = 32;
constexpr int L_Q = 0;
constexpr int L_OP = 512;
constexpr int L_P1 = 5120;
constexpr int L_P2 = 7424;
constexpr int L_ST = 12032;
constexpr int L_WO = 12288;
constexpr int L_XN = 35328;
constexpr int L_WQ = 128000;
constexpr int L_GX = 52736;
constexpr int L_TR = 54272;
constexpr int TR_W = 9216, TR_W2 = 4608;
constexpr int LDS_BYTES = L_WQ + 512 * 40;

template <int CTRL> __device__ __forceinline__ float dpp(float x) {
  return __builtin_bit_cast(float, __builtin_amdgcn_mov_dpp(__builtin_bit_cast(int, x), CTRL, 0xf, 0xf, true));
}
constexpr int XOR1 = 0xB1, XOR2 = 0x4E, XOR7 = 0x141, ROR1 = 0x121, ROR2 = 0x122, ROR4 = 0x124, ROR8 = 0x128, BCAST = 0x150;
__device__ __forceinline__ float lo16(unsigned v) { return __uint_as_float(v << 16); }
__device__ __forceinline__ float hi16(unsigned v) { return __uint_as_float(v & 0xffff0000u); }
__device__ __forceinline__ unsigned pk2(float a, float b) {
  f32x2 p = {a, b};
  return __builtin_bit_cast(unsigned, __builtin_convertvector(p, bf16x2));
}
__device__ __forceinline__ float rbf(float a) { return lo16(pk2(a, a)); }
__device__ __forceinline__ float dot2(unsigned a, unsigned b, float c) {
  return __builtin_amdgcn_fdot2_f32_bf16(__builtin_bit_cast(bf16x2, a), __builtin_bit_cast(bf16x2, b), c, false);
}
__device__ __forceinline__ f32x4 mfma(v4u a, v4u b, f32x4 c) {
  return __builtin_amdgcn_mfma_f32_16x16x32_bf16(__builtin_bit_cast(bf16x8, a), __builtin_bit_cast(bf16x8, b), c, 0, 0, 0);
}
__device__ __forceinline__ float tr32(float a, float b) {
  auto s = __builtin_amdgcn_permlane32_swap(__float_as_uint(a), __float_as_uint(b), false, false);
  return __uint_as_float(s[0]) + __uint_as_float(s[1]);
}
__device__ __forceinline__ float tr16(float a, float b) {
  auto s = __builtin_amdgcn_permlane16_swap(__float_as_uint(a), __float_as_uint(b), false, false);
  return __uint_as_float(s[0]) + __uint_as_float(s[1]);
}
__device__ __forceinline__ float wave_sum(float x) {
  x += dpp<ROR8>(x); x += dpp<ROR4>(x); x += dpp<ROR2>(x); x += dpp<ROR1>(x);
  x = tr16(x, x); x = tr32(x, x);
  return x;
}
__device__ __forceinline__ float wave_max(float x) {
  x = fmaxf(x, dpp<ROR8>(x)); x = fmaxf(x, dpp<ROR4>(x)); x = fmaxf(x, dpp<ROR2>(x)); x = fmaxf(x, dpp<ROR1>(x));
  auto s = __builtin_amdgcn_permlane16_swap(__float_as_uint(x), __float_as_uint(x), false, false);
  x = fmaxf(__uint_as_float(s[0]), __uint_as_float(s[1]));
  auto t = __builtin_amdgcn_permlane32_swap(__float_as_uint(x), __float_as_uint(x), false, false);
  return fmaxf(__uint_as_float(t[0]), __uint_as_float(t[1]));
}
template <int CTRL> __device__ __forceinline__ float trstep(float lo, float hi, bool bit) {
  const float keep = bit ? hi : lo, send = bit ? lo : hi;
  return keep + dpp<CTRL>(send);
}
__device__ __forceinline__ rsrc_t mkrs(const void* p, int bytes) {
  return __builtin_amdgcn_make_buffer_rsrc((void*)p, 0, bytes, 0x00020000);
}
template <int AUX> __device__ __forceinline__ v4u ld128(rsrc_t rs, int voff, int soff) {
  return __builtin_amdgcn_raw_buffer_load_b128(rs, voff, soff, AUX);
}
__device__ __forceinline__ void lds_barrier() {
  asm volatile("s_waitcnt lgkmcnt(0)" ::: "memory");
  __builtin_amdgcn_s_barrier();
  asm volatile("" ::: "memory");
}
__device__ __forceinline__ void lds_barrier_if(int on) {
  asm volatile("s_waitcnt lgkmcnt(0)\n\ts_cmp_eq_u32 %0, 0\n\ts_cbranch_scc1 1f\n\ts_barrier\n1:" :: "s"(on) : "scc", "memory");
}
__device__ __forceinline__ bool has_sent(v4u v) { return (v.x == SENT) | (v.y == SENT) | (v.z == SENT) | (v.w == SENT); }
__device__ __forceinline__ unsigned nosent(unsigned v) { return v < SENT ? v : SENT - 1u; }
constexpr int brev4(int i) { return ((i & 1) << 3) | ((i & 2) << 1) | ((i & 4) >> 1) | ((i & 8) >> 3); }
__device__ __forceinline__ void pace(unsigned target) {
  while ((int)((unsigned)__builtin_amdgcn_s_memrealtime() - target) < 0) __builtin_amdgcn_s_sleep(1);
}
__device__ __forceinline__ v4u trfrag(LAS unsigned char* p, int rs4) {
  const v2u u0 = __builtin_bit_cast(v2u, __builtin_amdgcn_ds_read_tr16_b64_v4i16((LAS v4s*)p));
  const v2u u1 = __builtin_bit_cast(v2u, __builtin_amdgcn_ds_read_tr16_b64_v4i16((LAS v4s*)(p + rs4)));
  return v4u{u0.x, u0.y, u1.x, u1.y};
}
__device__ __forceinline__ void repoll128(v4u& a, rsrc_t rs, int off) {
  _Pragma("nounroll") for (unsigned spins = 0; spins < SPIN_CAP; ++spins) {
    const bool bad = has_sent(a);
    if (__builtin_amdgcn_ballot_w64(bad) == 0ull) break;
    asm volatile("" : "+v"(off) :: "memory");
    if (bad) a = ld128<AUX_SC1>(rs, off, 0);
  }
}
__device__ __forceinline__ float half_sum(float x) {
  x += dpp<ROR8>(x); x += dpp<ROR4>(x); x += dpp<ROR2>(x); x += dpp<ROR1>(x);
  return tr16(x, x);
}

template <int I> __device__ __forceinline__ void axpy8(float (&acc)[8], float pv, v4u v) {
  const float p = dpp<BCAST + I>(pv);
#pragma unroll
  for (int k = 0; k < 4; ++k) {
    acc[2 * k] = fmaf(p, lo16(v[k]), acc[2 * k]);
    acc[2 * k + 1] = fmaf(p, hi16(v[k]), acc[2 * k + 1]);
  }
}
__device__ __forceinline__ void pin8(float (&a)[8]) {
  asm volatile("" : "+v"(a[0]), "+v"(a[1]), "+v"(a[2]), "+v"(a[3]), "+v"(a[4]), "+v"(a[5]), "+v"(a[6]), "+v"(a[7]));
}
__device__ __forceinline__ void pin1(float& a) { asm volatile("" : "+v"(a)); }
constexpr int kissued(int i) { return (NQ2 + (i + 3) / 4 > i + DIST) ? NQ2 + (i + 3) / 4 : i + DIST; }
static_assert(RP0 + DIST + 7 * RPS <= 63, "the last MLP weight load is stored inside the stream");
static_assert(kissued(32) == 32 + DIST, "the distance is reached inside the K phase");
template <int I, int N, typename F> __device__ __forceinline__ void sfor(F&& f) {
  if constexpr (I < N) { f(std::integral_constant<int, I>{}); sfor<I + 1, N>(f); }
}

#define STAMP(i) do {} while (0)

extern "C" __global__ void __launch_bounds__(NTHR)
fused_block(const u16* __restrict__ x, const u16* __restrict__ kc, const u16* __restrict__ vc,
            const u16* __restrict__ wq, const u16* __restrict__ wo, const u16* __restrict__ w1,
            const u16* __restrict__ b1, const u16* __restrict__ w2, const u16* __restrict__ b2,
            u16* out, unsigned char* ws) {
  __shared__ __attribute__((aligned(16))) unsigned char smem[LDS_BYTES];
  LAS unsigned char* L = (LAS unsigned char*)smem;
  unsigned long long tstart;
  asm volatile("s_memrealtime %0" : "=s"(tstart));
  const int tid = threadIdx.x, lane = tid & 63, w = (__builtin_amdgcn_readfirstlane(tid >> 6) + 8) % 9;
  const int r = lane >> 4, c = lane & 15;
  const int bid = blockIdx.x;
  const int kr = bid & 7, cg = bid >> 3;
  const int h = kr * 4 + (cg & 3), b = bid >> 7, s = (bid >> 5) & 3;
  const int bh = b * NH + h;
  const int l16 = lane * 16;
  asm volatile("" :: "s"(x), "s"(kc), "s"(vc), "s"(wq), "s"(wo), "s"(w1), "s"(b1), "s"(w2), "s"(b2), "s"(out), "s"(ws));
  const size_t kvoff = ((size_t)bh * SK + s * 1024 + w * 128) * DH;
  const rsrc_t rk = mkrs(kc + kvoff, w < 8 ? 32768 : 0);
  v4u kv[64];
#pragma unroll
  for (int i = 0; i < NQ0; ++i) kv[i] = ld128<2>(rk, l16 + (i & 3) * 1024, (i & ~3) * 1024);
  __builtin_amdgcn_sched_barrier(0);
  asm volatile("s_waitcnt lgkmcnt(0)" : "+s"(tstart));
  const unsigned t0 = (unsigned)tstart;
  const rsrc_t rs = mkrs(ws, WS_BYTES);
  const bool is_m1 = EVENROLE ? ((kr & 1) == 0 && cg < 11 && (kr >> 1) * 11 + cg < M1_N) : (cg < 6 && kr * 6 + cg < M1_N);
  const bool is_m2 = bid >= M2_BASE && bid < M2_BASE + M2_N;
  const int mrole = is_m1 ? (EVENROLE ? (kr >> 1) * 11 + cg : kr * 6 + cg) : is_m2 ? bid - M2_BASE : 0;
  constexpr float LOG2E = 1.4426950408889634f;
  const f32x4 z4 = {0.f, 0.f, 0.f, 0.f};
  const v4u zero4 = {0u, 0u, 0u, 0u};
  const v4u ones = {0x3F803F80u, 0x3F803F80u, 0x3F803F80u, 0x3F803F80u};
  v4u ve[ENDV], gl = zero4, wov[2] = {zero4, zero4};
#pragma unroll
  for (int i = 0; i < ENDV; ++i) ve[i] = zero4;
  float acc[8], pr1 = 0.f, mw = 0.f, biasf = 0.f;
#pragma unroll
  for (int k = 0; k < 8; ++k) acc[k] = 0.f;
  unsigned tagA, tagB, xo = 0u;
  const int lse_off = OFF_LSE + (bh * 32 + (lane & 31)) * 16;
  const int ap_rep = (c & 7) + 8 * (r >> 1);
  const int ap_st = ap_rep < REPA ? OFF_AP + ap_rep * 16384 + ((cg >> 1) * 16 + kr * 2 + (r & 1)) * 64 + (cg & 1) * 32 + (c >> 3) * 16 : OOB;
  const int tw = (lane >> 1) * 40 + (lane & 1) * 16, tra = (8 * r + (c >> 2)) * 40 + (c & 3) * 8;
  if (__builtin_expect(w < 8, 1)) {
    const rsrc_t rv = mkrs(vc + kvoff, 32768);
    const rsrc_t ro = mkrs(wo + (size_t)(kr * 512 + w * 64) * DM + cg * 16, 64 * DM * 2 - cg * 32);
    const rsrc_t r1 = mkrs(w1 + (size_t)(64 * w) * FF + 32 * mrole, is_m1 ? 64 * FF * 2 - 64 * mrole : 0);
    const rsrc_t r2 = mkrs(w2 + 32 * mrole * DM + 64 * w, is_m1 ? 32 * DM * 2 - 128 * w : 0);
    v4u tv[8];
    auto issue = [&](auto N) {
      constexpr int n = N.value;
      if constexpr (n < 64) {
        if constexpr (TAU4 > 0 && n >= NQ0 && ((n - NQ0) & 1) == 0) pace(t0 + (unsigned)(n <= NSW ? (n - LEAD) * TAU4 / 4 : ((NSW - LEAD) * TAU4 + (n - NSW) * TAU4B) / 4));
        if constexpr (n >= PFLY && n < kissued(32 - DIST)) asm volatile("s_waitcnt vmcnt(%0)" :: "i"(PFLY - 1));
        if constexpr (n < 32) kv[n] = ld128<2>(rk, l16, n * 1024); else kv[n] = ld128<2>(rv, l16, (n - 32) * 1024);
        if constexpr (n >= RP0 && (n - RP0) % RPS == 0 && (n - RP0) / RPS < 8) {
          constexpr int j = (n - RP0) / RPS;
          if constexpr (j & 1) tv[j] = ld128<W_AUX>(r2, (lane >> 3) * (DM * 2) + (lane & 7) * 16, (j >> 1) * 8 * DM * 2);
          else tv[j] = ld128<W1_AUX>(r1, (lane >> 2) * (FF * 2) + (lane & 3) * 16, (j >> 1) * 16 * FF * 2);
        }
        if constexpr (n >= RP0 + DIST && (n - RP0 - DIST) % RPS == 0 && (n - RP0 - DIST) / RPS < 8) {
          constexpr int j = (n - RP0 - DIST) / RPS;
          LAS unsigned char* T = L + L_TR + w * TR_W;
          if constexpr (j & 1) *(LAS v4u*)(T + TR_W2 + ((j >> 1) * 8 + (lane >> 3)) * 144 + (lane & 7) * 16) = tv[j];
          else {
            LAS unsigned char* d = T + ((j >> 1) * 16 + (lane >> 2)) * 72 + (lane & 3) * 16;
            *(LAS v2u*)d = v2u{tv[j].x, tv[j].y}; *(LAS v2u*)(d + 8) = v2u{tv[j].z, tv[j].w};
          }
        }
        if constexpr (WOBAR && n == 63) { __builtin_amdgcn_sched_barrier(0); __builtin_amdgcn_s_barrier(); __builtin_amdgcn_sched_barrier(0); }
        if constexpr (n == WOP0) wov[0] = ld128<W_AUX>(ro, (lane >> 1) * (DM * 2) + (lane & 1) * 16, 0);
        if constexpr (n == WOP1) wov[1] = ld128<W_AUX>(ro, (lane >> 1) * (DM * 2) + (lane & 1) * 16, 32 * DM * 2);
        __builtin_amdgcn_sched_barrier(0);
      }
    };
    __builtin_amdgcn_s_barrier();
    asm volatile("" ::: "memory");
    sfor<NQ0, NQ2>(issue);
    _Pragma("nounroll") for (unsigned spins = 0; spins < (SPIN_CAP << 6); ++spins) {
      if (__builtin_amdgcn_readfirstlane(*(volatile LAS unsigned*)(L + L_Q + 264)) != 0u) break;
      __builtin_amdgcn_s_sleep(1);
    }
    asm volatile("" ::: "memory");
    const v4u q = *(LAS v4u*)(L + L_Q + c * 16);
    {
      const v2u tg = *(LAS v2u*)(L + L_Q + 256);
      tagA = __builtin_amdgcn_readfirstlane(tg.x); tagB = __builtin_amdgcn_readfirstlane(tg.y);
    }
    STAMP(1);
    __builtin_amdgcn_sched_barrier(0);
    float sc[2];
    {
      const bool b3 = c & 8, b2_ = c & 4, b1_ = c & 2, b0 = c & 1;
      float la, lb, lc, ld;
      sfor<0, 32>([&](auto I) {
        constexpr int i = I.value;
        const v4u kk = kv[i];
        float d = dot2(kk.x, q.x, 0.f);
        d = dot2(kk.y, q.y, d); d = dot2(kk.z, q.z, d); d = dot2(kk.w, q.w, d);
        if constexpr ((i & 1) != 0) {
          d = trstep<ROR8>(la, d, b3);
          if constexpr ((i & 2) != 0) {
            d = trstep<XOR7>(lb, d, b2_);
            if constexpr ((i & 4) != 0) {
              d = trstep<XOR2>(lc, d, b1_);
              if constexpr ((i & 8) != 0) d = trstep<XOR1>(ld, d, b0);
            }
          }
        }
        pin1(d);
        if constexpr ((i & 1) == 0) la = d; else if constexpr ((i & 2) == 0) lb = d; else if constexpr ((i & 4) == 0) lc = d;
        else if constexpr ((i & 8) == 0) ld = d; else sc[i >> 4] = d;
        __builtin_amdgcn_sched_barrier(0);
        sfor<kissued(i), kissued(i + 1)>(issue);
      });
    }
    STAMP(2);
    float pr[2];
    {
      const float s0 = rbf(rbf(sc[0]) * 0.08838834764831845f), s1 = rbf(rbf(sc[1]) * 0.08838834764831845f);
      mw = wave_max(fmaxf(s0, s1));
      pr[0] = __builtin_amdgcn_exp2f((s0 - mw) * LOG2E);
      pr[1] = __builtin_amdgcn_exp2f((s1 - mw) * LOG2E);
      const float lw = wave_sum(pr[0] + pr[1]);
      __builtin_amdgcn_raw_buffer_store_b128(v4u{__float_as_uint(mw), tagA, __float_as_uint(lw), tagB}, rs,
                                             lane == 0 ? OFF_LSE + ((bh * 4 + s) * 8 + w) * 16 : OOB, 0, AUX_SC1);
    }
    __builtin_amdgcn_sched_barrier(0);
    sfor<0, 32 - DIST>([&](auto J) {
      constexpr int j = J.value;
      constexpr int c0 = (j * (32 - ENDV) + 31 - DIST) / (32 - DIST), c1 = ((j + 1) * (32 - ENDV) + 31 - DIST) / (32 - DIST);
      sfor<c0, c1>([&](auto I) {
        constexpr int i = I.value;
        axpy8<brev4(i & 15)>(acc, pr[i >> 4], kv[32 + i]);
        pin8(acc);
        __builtin_amdgcn_sched_barrier(0);
      });
      issue(std::integral_constant<int, 32 + j + DIST>{});
      if constexpr (j == 32 - DIST - ENDV - 1) {
        gl = ld128<AUX_SC1>(rs, lse_off, 0);
        __builtin_amdgcn_sched_barrier(0);
      }
    });
#pragma unroll
    for (int i = 0; i < ENDV; ++i) ve[i] = kv[64 - ENDV + i];
    pr1 = pr[1];
  } else {
    __builtin_amdgcn_s_setprio(3);
    v4u cpf = zero4;
    if (CPF) {
      unsigned long long pc0; unsigned back, len;
      asm volatile("s_getpc_b64 %0\n.Lfb_here:\n\ts_mov_b32 %1, .Lfb_here - fused_block\n\ts_mov_b32 %2, .Lfb_end - fused_block"
                   : "=s"(pc0), "=s"(back), "=s"(len));
      pc0 -= back;
      cpf = ld128<0>(mkrs((const void*)pc0, (int)len), cg * 1024 + l16, 0);
    }
    const rsrc_t rx = mkrs(x, NB * DM * 2);
    const v2u e = __builtin_amdgcn_raw_buffer_load_b64(rs, OFF_EPOCH, 0, 0);
    const v4u xs0 = ld128<0>(rx, lane * 32, 0), xs1 = ld128<0>(rx, lane * 32 + 16, 0);
    v4u wr[16];
    const int xbo = ((c & 1) * DM + 64 * kr + 8 * r) * 2;
    const v4u xb0 = ld128<0>(rx, xbo, 0), xb1 = ld128<0>(rx, xbo + 64, 0);
    {
      const rsrc_t rq = mkrs(wq + (size_t)(64 * kr) * (NH * DH) + cg * DH, 64 * NH * DH * 2 - cg * DH * 2);
#pragma unroll
      for (int j = 0; j < 16; ++j) wr[j] = ld128<2>(rq, r * (NH * DH * 2) + c * 16, j * 4 * NH * DH * 2);
    }
    *(LAS unsigned*)(L + L_Q + 264) = 0u;
    lds_barrier();
    STAMP(13);
    {
      static_assert(REPA <= 6, "one store covers the replicas");
      const int l8 = (lane - 16) >> 3;
      const int c0 = lane < 16 ? OFF_OP + (h * 8 + b * 4 + s) * 256 + l16
                   : ((cg & 1) == 0 && l8 < REPA) ? OFF_AP + l8 * 16384 + ((cg >> 1) * 16 + kr * 2) * 64 + (lane & 7) * 16 : OOB;
      __builtin_amdgcn_raw_buffer_store_b128(v4u{SENT, SENT, SENT, SENT}, rs, c0, 0, AUX_SC1);
      const int co = is_m1 ? OFF_PO + (lane >> 3) * PO_CG + mrole * 128 + (lane & 7) * 16 : OOB;
#pragma unroll
      for (int k = 0; k < 4; ++k) __builtin_amdgcn_raw_buffer_store_b128(v4u{SENT, SENT, SENT, SENT}, rs, co, k * 8 * PO_CG, AUX_SC1);
    }
    __builtin_amdgcn_sched_barrier(0);
    v4u xn0, xn1;
    {
      float sm = 0.f;
#pragma unroll
      for (int k = 0; k < 4; ++k) { sm = dot2(xs0[k], 0x3F803F80u, sm); sm = dot2(xs1[k], 0x3F803F80u, sm); }
      const float mean = half_sum(sm) * (1.f / DM);
      float sq = 0.f;
#pragma unroll
      for (int k = 0; k < 4; ++k) {
        const float d0 = lo16(xs0[k]) - mean, d1 = hi16(xs0[k]) - mean, d2 = lo16(xs1[k]) - mean, d3 = hi16(xs1[k]) - mean;
        sq = fmaf(d0, d0, sq); sq = fmaf(d1, d1, sq); sq = fmaf(d2, d2, sq); sq = fmaf(d3, d3, sq);
      }
      const float var = half_sum(sq) * (1.f / DM);
      const float mb = rbf(mean), rd = 1.f / rbf(__builtin_amdgcn_sqrtf(rbf(rbf(var) + 1e-5f)));
      const float m0 = __uint_as_float(__builtin_amdgcn_readlane(__float_as_uint(mb), 0)), m1 = __uint_as_float(__builtin_amdgcn_readlane(__float_as_uint(mb), 32));
      const float r0 = __uint_as_float(__builtin_amdgcn_readlane(__float_as_uint(rd), 0)), r1 = __uint_as_float(__builtin_amdgcn_readlane(__float_as_uint(rd), 32));
      const float mm = (c & 1) ? m1 : m0, rr = (c & 1) ? r1 : r0;
#pragma unroll
      for (int k = 0; k < 4; ++k) {
        const unsigned a0 = pk2(lo16(xb0[k]) - mm, hi16(xb0[k]) - mm), a1 = pk2(lo16(xb1[k]) - mm, hi16(xb1[k]) - mm);
        xn0[k] = pk2(lo16(a0) * rr, hi16(a0) * rr);
        xn1[k] = pk2(lo16(a1) * rr, hi16(a1) * rr);
      }
      const unsigned e0 = __builtin_amdgcn_readfirstlane(e.x), e1 = __builtin_amdgcn_readfirstlane(e.y);
      tagA = e0 + 1u;
      tagB = (e1 ^ (e0 * 0x9E3779B1u)) + 0x7F4A7C15u;
    }
    asm volatile("" : "+v"(xn0), "+v"(xn1) : "v"(cpf));
    STAMP(14);
    {
#pragma unroll
      for (int j = 0; j < 16; ++j) *(LAS v4u*)(L + L_WQ + (4 * j + r) * 272 + c * 16) = wr[j];
      const int ra = L_WQ + (8 * r + (c >> 2)) * 272 + (c & 3) * 8;
      f32x4 d = z4;
#pragma unroll
      for (int n = 0; n < 8; ++n) {
        const f32x4 dn = mfma(trfrag(L + ra + 32 * 272 + n * 32, 4 * 272), xn1, mfma(trfrag(L + ra + n * 32, 4 * 272), xn0, z4));
        if ((c >> 1) == n) d = dn;
      }
      const int qo = OFF_QX + (((cg * 2 + (c & 1)) * 8 + kr) * 64 + (c >> 1) * 4 + r) * 16;
      __builtin_amdgcn_raw_buffer_store_b128(v4u{__float_as_uint(d.x), tagA, __float_as_uint(d.y), tagB}, rs, qo, 0, AUX_SC1);
      __builtin_amdgcn_raw_buffer_store_b128(v4u{__float_as_uint(d.z), tagA, __float_as_uint(d.w), tagB}, rs, qo + 512, 0, AUX_SC1);
      STAMP(12);
      asm volatile("s_waitcnt vmcnt(0)" ::: "memory");
      __builtin_amdgcn_raw_buffer_store_b64(v2u{tagA, tagB}, rs, lane == 0 ? OFF_FLAG + (kr * 32 + cg) * 8 : OOB, 0, AUX_SC1);
    }
    {
      const int qx_off = OFF_QX + (h * 2 + b) * 8192 + l16;
      v4u qx[8];
      pace(t0 + QWAIT);
      _Pragma("nounroll") for (unsigned spins = 0; spins < SPIN_CAP; ++spins) {
        asm volatile("" ::: "memory");
        bool bad = false;
#pragma unroll
        for (int k = 0; k < 8; ++k) qx[k] = ld128<AUX_SC1>(rs, qx_off, k * 1024);
#pragma unroll
        for (int k = 0; k < 8; ++k) bad |= (qx[k].y != tagA) | (qx[k].w != tagB);
        if (__builtin_amdgcn_ballot_w64(bad) == 0ull) break;
        __builtin_amdgcn_s_sleep(16);
      }
      float q0 = 0.f, q1 = 0.f;
#pragma unroll
      for (int k = 0; k < 8; ++k) { q0 += __uint_as_float(qx[k].x); q1 += __uint_as_float(qx[k].z); }
      *(LAS unsigned*)(L + L_Q + (lane & 31) * 8 + (lane >> 5) * 4) = pk2(q0, q1);
      *(LAS v2u*)(L + L_Q + 256) = v2u{tagA, tagB};
      asm volatile("s_waitcnt lgkmcnt(0)" ::: "memory");
      *(volatile LAS unsigned*)(L + L_Q + 264) = 1u;
    }
    STAMP(15);
    if (is_m1) {
      *(LAS float*)(L + L_Q + 272 + (lane & 31) * 4) = lo16(__builtin_amdgcn_raw_buffer_load_b16(mkrs(b1 + 32 * mrole, 64), (lane & 31) * 2, 0, 0));
    }
    if (is_m2) biasf = lo16(__builtin_amdgcn_raw_buffer_load_b16(mkrs(b2 + 16 * mrole, 32), ((lane & 3) * 4 + (lane >> 5) * 2 + (r & 1)) * 2, 0, 0));
    xo = __builtin_amdgcn_raw_buffer_load_b32(rx, kr == 0 ? ((r & 1) * DM + cg * 16 + (c & 14)) * 2 : OOB, 0, 0);
    {
      const int fo0 = OFF_FLAG + ((is_m1 || is_m2) ? l16 : kr * 256 + c * 16), fo1 = (is_m1 || is_m2) ? fo0 + 1024 : fo0;
      pace(t0 + FWAIT + cg * 8);
      _Pragma("nounroll") for (unsigned spins = 0; spins < SPIN_CAP; ++spins) {
        asm volatile("" ::: "memory");
        const v4u fl0 = ld128<AUX_SC1>(rs, fo0, 0), fl1 = ld128<AUX_SC1>(rs, fo1, 0);
        if (__builtin_amdgcn_ballot_w64(fl0.x != tagA || fl0.y != tagB || fl0.z != tagA || fl0.w != tagB ||
                                        fl1.x != tagA || fl1.y != tagB || fl1.z != tagA || fl1.w != tagB) == 0ull) break;
        __builtin_amdgcn_s_sleep(32);
      }
    }
    asm volatile("" : "+v"(xo), "+v"(biasf));
    STAMP(2);
  }

  int pass = DRY ? (w ^ 8) : 1;
  asm volatile("" : "+s"(pass));
#pragma nounroll
  for (;;) {
    const bool doA = w < 8 || !pass, doB = w == 8;
    const rsrc_t rt = mkrs(ws, pass ? WS_BYTES : 0);
    if (doA) {
      if (pass && __builtin_amdgcn_ballot_w64(gl.y != tagA || gl.w != tagB) != 0ull) {
        _Pragma("nounroll") for (unsigned spins = 0; spins < SPIN_CAP; ++spins) {
          asm volatile("" ::: "memory");
          gl = ld128<AUX_SC1>(rs, lse_off, 0);
          if (__builtin_amdgcn_ballot_w64(gl.y != tagA || gl.w != tagB) == 0ull) break;
        }
      }
      const float m_i = __uint_as_float(gl.x), l_i = __uint_as_float(gl.z);
      const float M = wave_max(m_i);
      const float Ls = wave_sum(l_i * __builtin_amdgcn_exp2f((m_i - M) * LOG2E)) * 0.5f;
      float alpha = __builtin_amdgcn_exp2f((mw - M) * LOG2E) / Ls;
      pin1(alpha);
      __builtin_amdgcn_sched_barrier(0);
      sfor<0, ENDV>([&](auto I) {
        constexpr int i = I.value;
        asm volatile("" : "+v"(ve[i]));
        axpy8<brev4((32 - ENDV + i) & 15)>(acc, pr1, ve[i]);
        pin8(acc);
        __builtin_amdgcn_sched_barrier(0);
      });
      STAMP(3);
      const float t0_ = tr32(acc[0], acc[4]), t1 = tr32(acc[1], acc[5]), t2 = tr32(acc[2], acc[6]), t3 = tr32(acc[3], acc[7]);
      f32x2 o2 = {tr16(t0_, t2) * alpha, tr16(t1, t3) * alpha};
      *(LAS f32x2*)(L + L_OP + (w * DH + 8 * c + 2 * r) * 4) = o2;
    }
    lds_barrier_if(pass);
    if (doB) {
      f32x2 o2 = {0.f, 0.f};
#pragma unroll
      for (int ww = 0; ww < 8; ++ww) o2 += *(LAS f32x2*)(L + L_OP + (ww * DH + 2 * lane) * 4);
      const int pw = (int)nosent(pk2(o2.x, o2.y));
      const v4u pv = {(unsigned)pw, (unsigned)__builtin_amdgcn_mov_dpp(pw, 0x55, 0xf, 0xf, true), (unsigned)__builtin_amdgcn_mov_dpp(pw, 0xAA, 0xf, 0xf, true),
                      (unsigned)__builtin_amdgcn_mov_dpp(pw, 0xFF, 0xf, 0xf, true)};
      __builtin_amdgcn_raw_buffer_store_b128(pv, rt, (lane & 3) == 0 ? OFF_OP + (h * 8 + b * 4 + s) * 256 + lane * 4 : OOB, 0, AUX_SC1);
      STAMP(4);
    }
    v4u bf[8];
#pragma unroll
    for (int j = 0; j < 8; ++j) bf[j] = zero4;
    float cs = 0.f;
    if (doA) {
      if (is_m1) {
        LAS unsigned char* T = L + L_TR + (w & 7) * TR_W;
#pragma unroll
        for (int j = 0; j < 4; ++j) {
          bf[j] = trfrag(T + (8 * r + (c >> 2)) * 72 + (c & 3) * 8 + (j >> 1) * 32 * 72 + (j & 1) * 32, 288);
          bf[4 + j] = trfrag(T + TR_W2 + (8 * r + (c >> 2)) * 144 + (c & 3) * 8 + j * 32, 576);
        }
        const f32x4 c0 = mfma(ones, bf[2], mfma(ones, bf[0], z4)), c1 = mfma(ones, bf[3], mfma(ones, bf[1], z4));
        cs = r < 2 ? c0.x : c1.x;
        biasf = *(LAS float*)(L + L_Q + 272 + ((r >> 1) * 16 + c) * 4);
        pin1(cs); pin1(biasf);
      }
      __builtin_amdgcn_sched_barrier(0);
      const int po = OFF_OP + (((kr * 4 + ((w & 7) >> 1)) * 8 + (c & 7)) * DH + 64 * (w & 1) + 32 * (c >> 3) + 8 * r) * 2;
      if (D1) __builtin_amdgcn_s_sleep(D1);
      v4u a = ld128<AUX_SC1>(rt, po, 0);
      __builtin_amdgcn_sched_barrier(0);
      LAS unsigned char* T = L + L_WO + w * 2560;
      v4u bo[2];
#pragma unroll
      for (int j = 0; j < 2; ++j) {
        *(LAS v2u*)(T + tw + j * 1280) = v2u{wov[j].x, wov[j].y};
        *(LAS v2u*)(T + tw + j * 1280 + 8) = v2u{wov[j].z, wov[j].w};
        bo[j] = trfrag(T + tra + j * 1280, 160);
      }
      repoll128(a, rt, po);
      STAMP(5);
      const f32x4 d0 = mfma(a, bo[0], z4), d1 = mfma(a, bo[1], z4);
      const f32x4 d = r < 2 ? d0 : d1;
      *(LAS float*)(L + L_P1 + (w * 64 + lane) * 4) = (d.x + d.y) + (d.z + d.w);
    }
    lds_barrier_if(pass);
    if (doB) {
      float u = 0.f;
#pragma unroll
      for (int ww = 0; ww < 8; ++ww) u += *(LAS float*)(L + L_P1 + (ww * 64 + lane) * 4);
      u = tr32(u, u) + ((c & 1) ? hi16(xo) : lo16(xo));
      const unsigned p01 = nosent(pk2(dpp<0x00>(u), dpp<0x55>(u))), p23 = nosent(pk2(dpp<0xAA>(u), dpp<0xFF>(u)));
      const unsigned o01 = __builtin_amdgcn_mov_dpp(p01, XOR7, 0xf, 0xf, true), o23 = __builtin_amdgcn_mov_dpp(p23, XOR7, 0xf, 0xf, true);
      const v4u pv = (c & 4) ? v4u{o01, o23, p01, p23} : v4u{p01, p23, o01, o23};
      __builtin_amdgcn_raw_buffer_store_b128(pv, rt, ap_st, 0, AUX_SC1);
      STAMP(6);
    }
    if (is_m1) {
      if (doA) {
        const int ao = OFF_AP + (mrole % REPA) * 16384 + (w & 7) * 2048 + c * 64 + r * 16;
        if (D2) __builtin_amdgcn_s_sleep(D2);
        v4u a0 = ld128<AUX_SC1>(rt, ao, 0), a1 = ld128<AUX_SC1>(rt, ao + 1024, 0);
        _Pragma("nounroll") for (unsigned spins = 0; spins < SPIN_CAP; ++spins) {
          const bool bad = has_sent(a0) || has_sent(a1);
          if (__builtin_amdgcn_ballot_w64(bad) == 0ull) break;
          asm volatile("" ::: "memory");
          if (bad) { a0 = ld128<AUX_SC1>(rt, ao, 0); a1 = ld128<AUX_SC1>(rt, ao + 1024, 0); }
        }
        STAMP(7);
        f32x4 d0 = mfma(a0, bf[0], z4), d1 = mfma(a0, bf[1], z4), dg = mfma(a0, a0, z4), dsum = mfma(a0, ones, z4);
        d0 = mfma(a1, bf[2], d0); d1 = mfma(a1, bf[3], d1); dg = mfma(a1, a1, dg); dsum = mfma(a1, ones, dsum);
        const float u = tr16(tr32(d0.x + d0.z, d1.x + d1.z), tr32(d0.y + d0.w, d1.y + d1.w));
        float gq = (c & 1) ? dg.y + dg.w : dg.x + dg.z;
        gq += dpp<ROR2>(gq); gq += dpp<ROR4>(gq); gq += dpp<ROR8>(gq);
        const float sh = (c & 1) ? dsum.y + dsum.w : dsum.x + dsum.z;
        float st = tr32(gq, sh);
        st = tr16(st, st);
        *(LAS f32x2*)(L + L_P2 + (w * 64 + lane) * 8) = f32x2{u, cs};
        if ((lane & 30) == 0) *(LAS float*)(L + L_ST + (w < 8 ? (((c & 1) * 8 + w) * 2 + (r >> 1)) * 4 : 128)) = st;
      }
      lds_barrier_if(pass);
      if (doA) {
        f32x2 uc = {0.f, 0.f};
#pragma unroll
        for (int ww = 0; ww < 8; ++ww) uc += *(LAS f32x2*)(L + L_P2 + (ww * 64 + lane) * 8);
        f32x4 sv = z4;
#pragma unroll
        for (int k = 0; k < 4; ++k) sv += *(LAS f32x4*)(L + L_ST + (r & 1) * 64 + k * 16);
        const float mean = (sv.y + sv.w) * (1.f / DM);
        const float var = fmaxf((sv.x + sv.z) * (1.f / DM) - mean * mean, 0.f);
        const float rd = __builtin_amdgcn_rcpf(rbf(__builtin_amdgcn_sqrtf(rbf(rbf(var) + 1e-5f))));
        const float z = rbf(rbf((uc.x - rbf(mean) * uc.y) * rd) + biasf);
        const float inner = 0.7978845608028654f * (z + 0.044715f * z * z * z);
        const float g = z * __builtin_amdgcn_rcpf(1.f + __builtin_amdgcn_exp2f(-2.f * LOG2E * inner));
        *(LAS u16*)(L + L_GX + w * 128 + (r & 1) * 64 + ((r >> 1) * 16 + c) * 2) = (u16)pk2(g, g);
        const v4u gb = *(LAS v4u*)(L + L_GX + w * 128 + (c & 1) * 64 + r * 16);
        const f32x4 d0 = mfma(bf[4], gb, z4), d1 = mfma(bf[5], gb, z4), d2 = mfma(bf[6], gb, z4), d3 = mfma(bf[7], gb, z4);
        const f32x4 d = (c & 4) ? ((c & 2) ? d3 : d2) : ((c & 2) ? d1 : d0);
        const v4u dv = {nosent(__float_as_uint(d.x)), nosent(__float_as_uint(d.y)), nosent(__float_as_uint(d.z)), nosent(__float_as_uint(d.w))};
        __builtin_amdgcn_raw_buffer_store_b128(dv, rt, c < 8 ? OFF_PO + ((w & 7) * 4 + (c >> 1)) * PO_CG + mrole * 128 + (c & 1) * 64 + r * 16 : OOB, 0, AUX_SC1);
        STAMP(8);
      }
    } else if (is_m2) {
      if (doB) {
        const int ho = OFF_AP + (mrole % REPA) * 16384 + ((mrole >> 1) * 16 + (lane >> 2)) * 64 + (mrole & 1) * 32 + (lane & 3) * 8;
        if (D2) __builtin_amdgcn_s_sleep(D2);
        v2u hw;
        _Pragma("nounroll") for (unsigned spins = 0; spins < SPIN_CAP; ++spins) {
          asm volatile("" ::: "memory");
          hw = __builtin_amdgcn_raw_buffer_load_b64(rt, ho, 0, AUX_SC1);
          if (__builtin_amdgcn_ballot_w64(hw.x == SENT || hw.y == SENT) == 0ull) break;
        }
        float hv = tr16(tr32(lo16(hw.x), lo16(hw.y)), tr32(hi16(hw.x), hi16(hw.y)));
        hv = rbf(hv + dpp<ROR8>(hv));
        pin1(hv);
        STAMP(7);
        const int po = OFF_PO + mrole * PO_CG + l16, po5 = lane < 24 ? po : OOB;
        v4u p[6];
        if (D3) __builtin_amdgcn_s_sleep(D3);
        _Pragma("nounroll") for (unsigned spins = 0; spins < SPIN_CAP; ++spins) {
          asm volatile("" ::: "memory");
#pragma unroll
          for (int k = 0; k < 6; ++k) p[k] = ld128<AUX_SC1>(rt, k < 5 ? po : po5, k * 1024);
          bool bad = false;
#pragma unroll
          for (int k = 0; k < 6; ++k) bad |= has_sent(p[k]);
          if (__builtin_amdgcn_ballot_w64(bad) == 0ull) break;
        }
        STAMP(8);
        f32x4 sa = __builtin_bit_cast(f32x4, p[0]);
#pragma unroll
        for (int k = 1; k < 6; ++k) sa += __builtin_bit_cast(f32x4, p[k]);
        float u = tr16(tr32(sa.x, sa.z), tr32(sa.y, sa.w));
        u += dpp<ROR8>(u);
        const float o = hv + rbf(rbf(u) + biasf);
        const rsrc_t ro = mkrs(out, pass ? NB * DM * 2 : 0);
        __builtin_amdgcn_raw_buffer_store_b16((u16)pk2(o, o), ro,
            (lane & 8) == 0 ? (((lane >> 2) & 1) * DM + 16 * mrole + 4 * (lane & 3) + (lane >> 5) * 2 + (r & 1)) * 2 : OOB, 0, 0);
        __builtin_amdgcn_raw_buffer_store_b32(tagA, rt, (mrole == 0 && lane == 0) ? OFF_EPOCH : OOB, 0, AUX_SC1);
        STAMP(9);
      }
    }
    if (pass) break;
    pass = 1;
    asm volatile("" : "+s"(pass));
    __builtin_amdgcn_s_waitcnt(0x0F70);
    if (WOBAR) __builtin_amdgcn_s_barrier();
    STAMP(11);
  }
  asm volatile(".Lfb_end:");
}

extern "C" void kernel_launch(void* const* d_in, const int* in_sizes, int n_in,
                              void* d_out, int out_size, void* d_ws, size_t ws_size,
                              hipStream_t stream) {
  fused_block<<<NWG, NTHR, 0, stream>>>((const u16*)d_in[0], (const u16*)d_in[1], (const u16*)d_in[2],
                                        (const u16*)d_in[3], (const u16*)d_in[4], (const u16*)d_in[6],
                                        (const u16*)d_in[7], (const u16*)d_in[8], (const u16*)d_in[9],
                                        (u16*)d_out, (unsigned char*)d_ws);
}
```

```cpp
#include <hip/hip_runtime.h>
#include <stdint.h>

typedef unsigned short u16;
typedef unsigned v4u __attribute__((ext_vector_type(4)));
typedef unsigned v2u __attribute__((ext_vector_type(2)));
typedef short v4s __attribute__((ext_vector_type(4)));
typedef __bf16 bf16x2 __attribute__((ext_vector_type(2)));
typedef __bf16 bf16x8 __attribute__((ext_vector_type(8)));
typedef float f32x4 __attribute__((ext_vector_type(4)));
typedef float f32x2 __attribute__((ext_vector_type(2)));
typedef __amdgpu_buffer_rsrc_t rsrc_t;
#define LAS __attribute__((address_space(3)))

constexpr int NQ0 = 6;
constexpr int NQ2 = 32, KR = 5;
constexpr int PFLY = 6;
constexpr int DIST = 6;
constexpr int ENDV = 4;
constexpr int TAU4 = 156, NSW = 16, TAU4B = 116, LEAD = 2;
constexpr int PH = 7, PH0 = -36;
constexpr int D1 = 10, D2 = 44, D2A = 6, D3 = 34;
constexpr int QWAIT = 550, FWAIT = 1500, FST = 5;
constexpr int NPROBE = 3;
constexpr int SWZ = 2;

constexpr int NB = 2, DM = 512, NH = 32, SK = 4096, DH = 128, FF = 1376;
constexpr int NWG = 256, NTHR = 576;
constexpr int NU = 33, NL = 2 * NU;
constexpr int NJ = NL - 6;
constexpr int OFF_EPOCH = 0;
constexpr int OFF_FX = 4096;
constexpr int OFF_QX = 16384;
constexpr int OFF_OP = OFF_QX + 16 * 16384;
constexpr int OFF_AP = OFF_OP + 8 * 16384;
constexpr int OFF_PO = OFF_AP + 2 * 16384;
constexpr int PO_CG = 44 * 32;
constexpr int WS_BYTES = OFF_PO + 32 * 16384;
constexpr int AUX_SC1 = 16, AUX_NT = 2;
constexpr int OOB = 0x7ffffff0;
constexpr unsigned SENT = 0xFFFFFFFFu;
constexpr unsigned SPIN_CAP = 1u << 19;
constexpr int M1_N = 43;
constexpr int M2_BASE = 64, M2_N = 32;
constexpr int L_Q = 0;
constexpr int L_OP = 512;
constexpr int L_P1 = 11520;
constexpr int L_P2 = 6656;
constexpr int L_CS = 9728;
constexpr int L_ST = 11264;
constexpr int L_NT = 15616;
constexpr int L_WQ = 16640;
constexpr int L_XN = 35072;
constexpr int L_ML = 416;
constexpr int L_TR = 49408;
constexpr int TR_W = 9216, TR_W2 = 4608;
constexpr int LDS_BYTES = L_TR + 8 * TR_W;

template <int CTRL> __device__ __forceinline__ unsigned dppu(unsigned x) {
  return (unsigned)__builtin_amdgcn_mov_dpp((int)x, CTRL, 0xf, 0xf, true);
}
template <int CTRL> __device__ __forceinline__ float dpp(float x) { return __uint_as_float(dppu<CTRL>(__float_as_uint(x))); }
constexpr int XOR1 = 0xB1, XOR2 = 0x4E, XOR7 = 0x141, ROR1 = 0x121, ROR2 = 0x122, ROR4 = 0x124, ROR8 = 0x128, ROR12 = 0x12C, BCAST = 0x150;
__device__ __forceinline__ float lo16(unsigned v) { return __uint_as_float(v << 16); }
__device__ __forceinline__ float hi16(unsigned v) { return __uint_as_float(v & 0xffff0000u); }
__device__ __forceinline__ unsigned pk2(float a, float b) {
  f32x2 p = {a, b};
  return __builtin_bit_cast(unsigned, __builtin_convertvector(p, bf16x2));
}
__device__ __forceinline__ float rbf(float a) { return __uint_as_float(pk2(0.f, a)); }
__device__ __forceinline__ float dot2(unsigned a, unsigned b, float c) {
  return __builtin_amdgcn_fdot2_f32_bf16(__builtin_bit_cast(bf16x2, a), __builtin_bit_cast(bf16x2, b), c, false);
}
__device__ __forceinline__ f32x4 mfma(v4u a, v4u b, f32x4 c) {
  return __builtin_amdgcn_mfma_f32_16x16x32_bf16(__builtin_bit_cast(bf16x8, a), __builtin_bit_cast(bf16x8, b), c, 0, 0, 0);
}
__device__ __forceinline__ float tr32(float a, float b) {
  auto s = __builtin_amdgcn_permlane32_swap(__float_as_uint(a), __float_as_uint(b), false, false);
  return __uint_as_float(s[0]) + __uint_as_float(s[1]);
}
__device__ __forceinline__ float tr16(float a, float b) {
  auto s = __builtin_amdgcn_permlane16_swap(__float_as_uint(a), __float_as_uint(b), false, false);
  return __uint_as_float(s[0]) + __uint_as_float(s[1]);
}
__device__ __forceinline__ float half_sum(float x) {
  x += dpp<ROR8>(x); x += dpp<ROR4>(x); x += dpp<ROR2>(x); x += dpp<ROR1>(x);
  return tr16(x, x);
}
__device__ __forceinline__ float wave_sum(float x) { x = half_sum(x); return tr32(x, x); }
__device__ __forceinline__ float wave_max(float x) {
  x = fmaxf(x, dpp<ROR8>(x)); x = fmaxf(x, dpp<ROR4>(x)); x = fmaxf(x, dpp<ROR2>(x)); x = fmaxf(x, dpp<ROR1>(x));
  auto s = __builtin_amdgcn_permlane16_swap(__float_as_uint(x), __float_as_uint(x), false, false);
  x = fmaxf(__uint_as_float(s[0]), __uint_as_float(s[1]));
  auto t = __builtin_amdgcn_permlane32_swap(__float_as_uint(x), __float_as_uint(x), false, false);
  return fmaxf(__uint_as_float(t[0]), __uint_as_float(t[1]));
}
template <int CTRL> __device__ __forceinline__ float trstep(float lo, float hi, bool bit) {
  const float keep = bit ? hi : lo, send = bit ? lo : hi;
  return keep + dpp<CTRL>(send);
}
__device__ __forceinline__ rsrc_t mkrs(const void* p, int bytes) {
  return __builtin_amdgcn_make_buffer_rsrc((void*)p, 0, bytes, 0x00020000);
}
template <int AUX> __device__ __forceinline__ v4u ld128(rsrc_t rs, int voff, int soff) {
  return __builtin_amdgcn_raw_buffer_load_b128(rs, voff, soff, AUX);
}
__device__ __forceinline__ void lds_barrier() {
  asm volatile("s_waitcnt lgkmcnt(0)\n\ts_barrier" ::: "memory");
}
__device__ __forceinline__ void barrier_if(int off) {
  asm volatile("s_cmp_lg_u32 %0, 0\n\ts_cbranch_scc1 1f\n\ts_barrier\n1:" :: "s"(off) : "scc", "memory");
}
__device__ __forceinline__ bool has_sent(v4u v) { return max(max(v.x, v.y), max(v.z, v.w)) == SENT; }
__device__ __forceinline__ unsigned nosent(unsigned v) { return v < SENT ? v : SENT - 1u; }
__device__ __forceinline__ bool any_lane(bool p) { return __builtin_amdgcn_ballot_w64(p) != 0ull; }
constexpr int brev4(int i) { return ((i & 1) << 3) | ((i & 2) << 1) | ((i & 4) >> 1) | ((i & 8) >> 3); }
__device__ __forceinline__ void pace(unsigned target) {
  while ((int)((unsigned)__builtin_amdgcn_s_memrealtime() - target) < 0) __builtin_amdgcn_s_sleep(1);
}
__device__ __forceinline__ v4u trfrag(LAS unsigned char* p, int rs4) {
  const v2u u0 = __builtin_bit_cast(v2u, __builtin_amdgcn_ds_read_tr16_b64_v4i16((LAS v4s*)p));
  const v2u u1 = __builtin_bit_cast(v2u, __builtin_amdgcn_ds_read_tr16_b64_v4i16((LAS v4s*)(p + rs4)));
  return v4u{u0.x, u0.y, u1.x, u1.y};
}
__device__ __forceinline__ void repoll1(v4u& a, rsrc_t rs, int off) {
  _Pragma("nounroll") for (unsigned spins = 0; spins < SPIN_CAP; ++spins) {
    const bool bad = has_sent(a);
    if (!any_lane(bad)) break;
    asm volatile("" : "+v"(off) :: "memory");
    if (bad) a = ld128<AUX_SC1>(rs, off, 0);
  }
}
__device__ __forceinline__ void poll2(v4u& a0, v4u& a1, rsrc_t rs, int off0, int off1) {
  a0 = ld128<AUX_SC1>(rs, off0, 0); a1 = ld128<AUX_SC1>(rs, off1, 0);
  _Pragma("nounroll") for (unsigned spins = 0; spins < SPIN_CAP; ++spins) {
    const bool bad = has_sent(a0) || has_sent(a1);
    if (!any_lane(bad)) break;
    asm volatile("" : "+v"(off0), "+v"(off1) :: "memory");
    if (bad) { a0 = ld128<AUX_SC1>(rs, off0, 0); a1 = ld128<AUX_SC1>(rs, off1, 0); }
  }
}
template <int I> __device__ __forceinline__ void axpy8(float (&acc)[8], float pv, v4u v) {
  const float p = dpp<BCAST + I>(pv);
#pragma unroll
  for (int k = 0; k < 4; ++k) {
    acc[2 * k] = fmaf(p, lo16(v[k]), acc[2 * k]);
    acc[2 * k + 1] = fmaf(p, hi16(v[k]), acc[2 * k + 1]);
  }
}
__device__ __forceinline__ void pin8(float (&a)[8]) {
  asm volatile("" : "+v"(a[0]), "+v"(a[1]), "+v"(a[2]), "+v"(a[3]), "+v"(a[4]), "+v"(a[5]), "+v"(a[6]), "+v"(a[7]));
}
template <typename T> __device__ __forceinline__ void pin(T& a) { asm volatile("" : "+v"(a)); }
constexpr int kissued(int i) { return (NQ2 + (i + KR - 1) / KR > i + DIST) ? NQ2 + (i + KR - 1) / KR : i + DIST; }
constexpr int vdone(int n) { return n + 1 - NU - DIST > NU - ENDV - 2 * (NL - 1 - n) ? n + 1 - NU - DIST : NU - ENDV - 2 * (NL - 1 - n); }
constexpr int shalf(int n) { return 2 * (n - LEAD); }
constexpr unsigned sched(int n) { return (unsigned)(n <= NSW ? shalf(n) * TAU4 / 8 : (shalf(NSW) * TAU4 + (shalf(n) - shalf(NSW)) * TAU4B) / 8); }
constexpr int vunit(int i) { return NU - 1 - i; }
constexpr int rpos(int j) { return 31 + j; }
constexpr int ridx(int n) { for (int j = 0; j < 4; ++j) if (rpos(j) == n) return j; return -1; }
constexpr int rfly(int n) { int k = 0; for (int j = 0; j < 4; ++j) k += rpos(j) < n && rpos(j) >= n - PFLY; return k; }
static_assert(kissued(NU) == NU + DIST && vdone(NL - 1) == NU - ENDV, "stream schedule");
static_assert(rpos(3) + DIST < NJ, "the MLP weights are in LDS when their operands are set up");
template <int I, int N, typename F> __device__ __forceinline__ void sfor(F&& f) {
  if constexpr (I < N) { f(std::integral_constant<int, I>{}); sfor<I + 1, N>(f); }
}

extern "C" __global__ void __launch_bounds__(NTHR, 3)
fused_block(const u16* __restrict__ x, const u16* __restrict__ kc, const u16* __restrict__ vc,
            const u16* __restrict__ wq, const u16* __restrict__ wo, const u16* __restrict__ w1,
            const u16* __restrict__ b1, const u16* __restrict__ w2, const u16* __restrict__ b2,
            u16* out, unsigned char* ws) {
  __shared__ __attribute__((aligned(16))) unsigned char smem[LDS_BYTES];
  LAS unsigned char* L = (LAS unsigned char*)smem;
  unsigned long long tstart;
  const int tid = threadIdx.x;
  int w = (__builtin_amdgcn_readfirstlane(tid) >> 6) - 1, bid = blockIdx.x;
  v4u kv[NL];
  {
    static_assert(SWZ == 2 && NQ0 == 6, "offsets below");
    const unsigned ko0 = (((bid & 7) << 22) | ((bid & 0x18) << 17) | ((bid & 0x60) << 13) | ((bid & 0x80) << 18) | ((w & 2) << 12)) | ((unsigned)w << 15);
    const int lo = (tid & 63) * 16;
    asm volatile("s_memrealtime %0" : "=s"(tstart) : "s"(x), "s"(kc), "s"(vc), "s"(wq), "s"(wo), "s"(w1), "s"(b1), "s"(w2), "s"(b2), "s"(out), "s"(ws), "s"(ko0), "v"(lo));
    const rsrc_t rka = mkrs(kc, NB * NH * SK * DH * 2);
    if (__builtin_expect(w >= 0, 1)) {
#pragma unroll
      for (int i = 0; i < NQ0; ++i) kv[i] = ld128<AUX_NT>(rka, lo + (i & 3) * 1024, i < 4 ? ko0 : ko0 + 4096);
    }
  }
  __builtin_amdgcn_sched_barrier(0);
  asm volatile("s_waitcnt lgkmcnt(0)" : "+s"(tstart), "+s"(w), "+s"(bid));
  const unsigned t0 = (unsigned)tstart;
  const int lane = tid & 63, r = lane >> 4, c = lane & 15;
  const int kr = bid & 7, cg = bid >> 3;
  const int h = kr * 4 + (cg & 3), b = bid >> 7, s = (bid >> 5) & 3;
  const int bh = b * NH + h;
  const int l16 = lane * 16;
  const int swz = (w & SWZ) ? 4096 : 0;
  const rsrc_t rs = mkrs(ws, WS_BYTES);
  const int gq = (cg & 3) + 4 * b, gi = kr * 6 + gq;
  const bool mlpg = gq < 6 && gi < M1_N;
  const int cnt = mlpg ? (s < 2 ? 31 : 33) : (s == 1 ? 30 : s == 3 ? 32 : 33);
  const bool is_m1 = mlpg && s == 0, is_t = s == 1;
  const bool is_m2 = bid >= M2_BASE && bid < M2_BASE + M2_N;
  const int mrole = is_m1 ? gi : is_m2 ? h : 0;
  constexpr float LOG2E = 1.4426950408889634f;
  const f32x4 z4 = {0.f, 0.f, 0.f, 0.f};
  const v4u ones = {0x3F803F80u, 0x3F803F80u, 0x3F803F80u, 0x3F803F80u};

  if (__builtin_expect(w >= 0, 1)) {
    const size_t kvoff = ((size_t)bh * SK + s * 1024 + w * 128) * DH;
    const rsrc_t rk = mkrs(kc + kvoff + swz, 24 * 1024), rv = mkrs(vc + kvoff + swz, 24 * 1024);
    const rsrc_t rk1 = mkrs(kc + kvoff - swz, min(cnt, 32) * 1024), rv1 = mkrs(vc + kvoff - swz, min(cnt, 32) * 1024);
    const int xs = mlpg ? s - 2 : 1, xu = mlpg ? 31 : 30 + (s >> 1);
    const size_t xoff = ((size_t)bh * SK + (cnt > 32 ? xs * 1024 + w * 128 + xu * 4 : 1024)) * DH - swz;
    const rsrc_t rkx = mkrs(kc + xoff, max(cnt - 32, 0) * 1024), rvx = mkrs(vc + xoff, max(cnt - 32, 0) * 1024);
    const rsrc_t rot = mkrs(wo + (size_t)(kr * 512 + w * 64) * DM + gq * 64, 64 * DM * 2 - gq * 128);
    const rsrc_t rA = is_t ? rot : mkrs(w1 + (size_t)(64 * w) * FF + 32 * mrole, is_m1 ? 64 * FF * 2 - 64 * mrole : 0);
    const rsrc_t rB = mkrs(w2 + 32 * mrole * DM + 64 * w, is_m1 ? 32 * DM * 2 - 128 * w : 0);
    const int voB = (lane >> 3) * (DM * 2) + (lane & 7) * 16, voA = is_t ? voB + 32 * DM * 2 : (lane >> 2) * (FF * 2) + (lane & 3) * 16;
    const int stA = is_t ? 8 * DM * 2 : 16 * FF * 2;
    const int lwB = L_TR + w * TR_W + (is_t ? 0 : TR_W2) + (lane >> 3) * 144 + (lane & 7) * 16;
    const int lwA = is_t ? lwB + TR_W2 : L_TR + w * TR_W + (lane >> 2) * 72 + (lane & 3) * 16;
    v4u tv[8];
    const unsigned tb = t0 + (unsigned)(w * PH + PH0);
    auto issue = [&](auto N) {
      constexpr int n = N.value;
      if constexpr (n >= PFLY && n < kissued(NU - DIST)) asm volatile("s_waitcnt vmcnt(%0)" :: "i"(PFLY - 1 + rfly(n)) : "memory");
      if constexpr (((n - NQ0) & 1) == 0) pace(tb + sched(n));
      constexpr int un = n < NU ? n : vunit(n - NU);
      if constexpr (un < 32) kv[n] = ld128<AUX_NT>((un & 8) ? (n < NU ? rk1 : rv1) : (n < NU ? rk : rv), l16, un * 1024);
      else kv[n] = ld128<AUX_NT>(n < NU ? rkx : rvx, l16, (un - 32) * 1024);
      if constexpr (ridx(n) >= 0) tv[ridx(n)] = ld128<AUX_NT>(rA, voA, ridx(n) * stA);
      if constexpr (ridx(n - DIST) >= 0) {
        constexpr int j = ridx(n - DIST);
        *(LAS v2u*)(L + lwA + j * 1152) = v2u{tv[j].x, tv[j].y}; *(LAS v2u*)(L + lwA + j * 1152 + 8) = v2u{tv[j].z, tv[j].w};
      }
      __builtin_amdgcn_sched_barrier(0);
    };
    __builtin_amdgcn_s_barrier();
    asm volatile("" ::: "memory");
    sfor<NQ0, NQ2>(issue);
    _Pragma("nounroll") for (unsigned spins = 0; spins < (SPIN_CAP << 6); ++spins) {
      if (__builtin_amdgcn_readfirstlane(*(volatile LAS unsigned*)(L + L_Q + 264)) != 0u) break;
      __builtin_amdgcn_s_sleep(1);
    }
    asm volatile("" ::: "memory");
    const v4u q = *(LAS v4u*)(L + L_Q + c * 16);
    __builtin_amdgcn_sched_barrier(0);
    float sc[3];
    {
      const bool b3 = c & 8, b2_ = c & 4, b1_ = c & 2, b0 = c & 1;
      float la, lb, lc, ld;
      sfor<0, NU>([&](auto I) {
        constexpr int i = I.value;
        const v4u kk = kv[i];
        float d = dot2(kk.x, q.x, 0.f);
        d = dot2(kk.y, q.y, d); d = dot2(kk.z, q.z, d); d = dot2(kk.w, q.w, d);
        if constexpr ((i & 1) != 0) {
          d = trstep<ROR8>(la, d, b3);
          if constexpr ((i & 2) != 0) {
            d = trstep<XOR7>(lb, d, b2_);
            if constexpr ((i & 4) != 0) {
              d = trstep<XOR2>(lc, d, b1_);
              if constexpr ((i & 8) != 0) d = trstep<XOR1>(ld, d, b0);
            }
          }
        }
        if constexpr (i == NU - 1) {
          d += dpp<ROR8>(d);
          d += dpp<XOR7>(d); d += dpp<XOR2>(d); d += dpp<XOR1>(d);
        }
        pin(d);
        if constexpr (i == NU - 1) sc[2] = d;
        else if constexpr ((i & 1) == 0) la = d; else if constexpr ((i & 2) == 0) lb = d; else if constexpr ((i & 4) == 0) lc = d;
        else if constexpr ((i & 8) == 0) ld = d; else sc[i >> 4] = d;
        __builtin_amdgcn_sched_barrier(0);
        sfor<kissued(i), kissued(i + 1)>(issue);
      });
    }
    float pr[3], mw;
    {
      const int ub = (int)(__builtin_bitreverse32((unsigned)c) >> 28);
      const float s0 = rbf(rbf(sc[0]) * 0.08838834764831845f);
      const float s1 = 16 + ub < cnt ? rbf(rbf(sc[1]) * 0.08838834764831845f) : -INFINITY;
      const float s2 = 32 + ub < cnt ? rbf(rbf(sc[2]) * 0.08838834764831845f) : -INFINITY;
      mw = wave_max(fmaxf(s0, fmaxf(s1, s2)));
      pr[0] = __builtin_amdgcn_exp2f((s0 - mw) * LOG2E);
      pr[1] = __builtin_amdgcn_exp2f((s1 - mw) * LOG2E);
      pr[2] = __builtin_amdgcn_exp2f((s2 - mw) * LOG2E);
      const float lw = wave_sum(pr[0] + pr[1] + pr[2]);
      *(volatile LAS unsigned long long*)(L + L_ML + w * 8) = __float_as_uint(mw) | ((unsigned long long)nosent(__float_as_uint(lw)) << 32);
    }
    __builtin_amdgcn_sched_barrier(0);
    float acc[8];
#pragma unroll
    for (int k = 0; k < 8; ++k) acc[k] = 0.f;
    auto vstep = [&](auto I) {
      constexpr int i = I.value;
      axpy8<brev4(vunit(i) & 15)>(acc, pr[vunit(i) >> 4], kv[NU + i]);
      pin8(acc);
      __builtin_amdgcn_sched_barrier(0);
    };
    sfor<NU + DIST, NJ>([&](auto N) {
      constexpr int n = N.value;
      sfor<vdone(n - 1), vdone(n)>(vstep);
      issue(N);
    });
    int a_op = L_OP + (w * DH + 8 * c + 2 * r) * 4;
    int a_po = OFF_OP + kr * 16384 + (((w >> 1) * 8 + (c & 7)) * DH + 64 * (w & 1) + 32 * (c >> 3) + 8 * r) * 2;
    int a_p1 = L_P1 + ((w * 2 + (r & 1)) * 64 + (r >> 1) * 32 + c) * 4;
    int a_g0 = is_m1 ? OFF_AP + (w >> 2) * 16384 + (w & 3) * 2048 + c * 64 + r * 16 : OFF_PO + mrole * 16384 + (w & 3) * PO_CG + l16;
    int a_g1 = is_m1 || lane < 22 ? a_g0 + 1024 : OOB - 8192;
    int a_ho = OFF_AP + (mrole >> 4) * 16384 + (((mrole >> 1) & 7) * 16 + c) * 64 + (mrole & 1) * 32 + (w & 3) * 8;
    int a_p2 = L_P2 + lane * 48 + w * 4;
    int a_st = L_ST + ((lane & 30) == 0 ? (((c & 1) * 8 + w) * 2 + (r >> 1)) * 4 : 128);
    int a_u = L_P2 + ((((r >> 1) * 2 + (c & 1)) * 16) + (r & 1) * 8 + (c >> 1)) * 48;
    int a_sv = L_ST + (c & 1) * 64 + r * 16;
    int a_pp = is_m1 ? ((c & 2) == 0 ? OFF_PO + (w * 4 + (c >> 2)) * 16384 + r * PO_CG + mrole * 32 + (c & 1) * 16 : OOB - 8192)
                     : (c < 2 ? (c * DM + 16 * mrole + 4 * (w & 3) + r) * 2 : OOB);
    v4u bf[8];
    int a_tb = L_TR + w * TR_W + (is_t ? 0 : TR_W2) + (8 * r + (c >> 2)) * 144 + (c & 3) * 8, a_lw = lwB;
    {
      const int ta = is_t ? a_tb + TR_W2 : L_TR + w * TR_W + (8 * r + (c >> 2)) * 72 + (c & 3) * 8;
      const int t4 = is_t ? 576 : 288, t2 = is_t ? 64 : 32 * 72;
#pragma unroll
      for (int j = 0; j < 4; ++j) bf[j] = trfrag(L + ta + (j >> 1) * t2 + (j & 1) * 32, t4);
      const f32x4 c0 = mfma(ones, bf[2], mfma(ones, bf[0], z4)), c1 = mfma(ones, bf[3], mfma(ones, bf[1], z4));
      *(LAS float*)(L + L_CS + ((r >> 1) * 16 + c) * 48 + w * 4) = r < 2 ? c0.x : c1.x;
    }
    float bias = *(LAS float*)(L + L_Q + 272 + (is_m1 ? 8 * r + (c >> 1) : 4 * (w & 3) + r) * 4);
    asm volatile("" : "+v"(a_op), "+v"(a_po), "+v"(a_p1), "+v"(a_tb), "+v"(a_lw), "+v"(a_g0), "+v"(a_g1), "+v"(a_ho), "+v"(a_p2), "+v"(a_st),
                 "+v"(a_u), "+v"(a_sv), "+v"(a_pp), "+v"(bias));
#pragma unroll
    for (int j = 0; j < 4; ++j) pin(bf[j]);
    __builtin_amdgcn_sched_barrier(0);
    sfor<NJ, NL>([&](auto N) {
      constexpr int n = N.value;
      sfor<vdone(n - 1), vdone(n)>(vstep);
      issue(N);
    });
    barrier_if(s ^ 1);
    {
      const rsrc_t rBt = mkrs(wo + (size_t)(kr * 512 + w * 64) * DM + gq * 64, is_t ? 64 * DM * 2 - gq * 128 : 0);
#pragma unroll
      for (int j = 0; j < 4; ++j) tv[4 + j] = ld128<AUX_NT>(rBt, voB, j * 8 * DM * 2);
    }
    __builtin_amdgcn_sched_barrier(0);
    sfor<NU - ENDV, NU>(vstep);
    {
      const float t0_ = tr32(acc[0], acc[4]), t1 = tr32(acc[1], acc[5]), t2 = tr32(acc[2], acc[6]), t3 = tr32(acc[3], acc[7]);
      *(LAS f32x2*)(L + a_op) = f32x2{tr16(t0_, t2), tr16(t1, t3)};
    }
    lds_barrier();
    {
      const int near = *(LAS int*)(L + L_Q + 268);
      a_g0 += near; a_g1 += near; a_ho += near;
      a_po += *(LAS int*)(L + L_NT + ((c & 7) * 4 + (w >> 1)) * 4) * 8192;
    }
    auto late_half = [&]() {
#pragma unroll
      for (int j = 0; j < 4; ++j) *(LAS v4u*)(L + a_lw + j * 1152) = tv[4 + j];
#pragma unroll
      for (int j = 0; j < 4; ++j) bf[4 + j] = trfrag(L + a_tb + j * 32, 576);
    };
    if (is_t) {
      __builtin_amdgcn_s_sleep(D1);
      v4u a = ld128<AUX_SC1>(rs, a_po, 0);
      __builtin_amdgcn_sched_barrier(0);
      late_half();
#pragma unroll
      for (int j = 0; j < 4; ++j) pin(bf[4 + j]);
      repoll1(a, rs, a_po);
      float v[4];
#pragma unroll
      for (int j = 0; j < 4; ++j) {
        const f32x4 da = mfma(a, bf[4 + j], z4), db = mfma(a, bf[j], z4);
        const f32x4 e = r < 2 ? da : db;
        v[j] = (e.x + e.y) + (e.z + e.w);
      }
      *(LAS float*)(L + a_p1) = tr32(v[0], v[2]);
      *(LAS float*)(L + a_p1 + 64) = tr32(v[1], v[3]);
      lds_barrier();
    } else if (is_m1) {
      __builtin_amdgcn_s_sleep(4);
#pragma unroll
      for (int j = 0; j < 4; ++j) tv[4 + j] = ld128<AUX_NT>(rB, voB, j * 8 * DM * 2);
      late_half();
#pragma unroll
      for (int j = 0; j < 4; ++j) {
        const v4u f = bf[4 + j];
        bf[4 + j] = v4u{f[j & 3], f[(j + 1) & 3], f[(j + 2) & 3], f[(j + 3) & 3]};
        pin(bf[4 + j]);
      }
      float cs;
      {
        const f32x4 c4 = *(LAS f32x4*)(L + L_CS + (8 * r + (c >> 1)) * 48) + *(LAS f32x4*)(L + L_CS + (8 * r + (c >> 1)) * 48 + 16);
        cs = (c4.x + c4.y) + (c4.z + c4.w);
        pin(cs);
      }
      {
        a_pp += *(LAS int*)(L + L_NT + (w * 32 + M2_BASE / 8 + (c >> 2)) * 4) * 8192;
        pin(a_pp);
      }
      __builtin_amdgcn_s_sleep(D2A);
      v4u a0, a1;
      poll2(a0, a1, rs, a_g0, a_g1);
      f32x4 dg = mfma(a0, a0, z4), dsum = mfma(a0, ones, z4), d0 = mfma(a0, bf[0], z4), d1 = mfma(a0, bf[1], z4);
      dg = mfma(a1, a1, dg); dsum = mfma(a1, ones, dsum); d0 = mfma(a1, bf[2], d0); d1 = mfma(a1, bf[3], d1);
      __builtin_amdgcn_sched_barrier(0);
      float gq = (c & 1) ? dg.y + dg.w : dg.x + dg.z;
      const float sh = (c & 1) ? dsum.y + dsum.w : dsum.x + dsum.z;
      gq += dpp<ROR2>(gq); gq += dpp<ROR4>(gq); gq += dpp<ROR8>(gq);
      const float u = tr16(tr32(d0.x + d0.z, d1.x + d1.z), tr32(d0.y + d0.w, d1.y + d1.w));
      float st = tr32(gq, sh);
      st = tr16(st, st);
      *(LAS float*)(L + a_p2) = u;
      *(LAS float*)(L + a_st) = st;
      lds_barrier();
      const f32x4 sv = *(LAS f32x4*)(L + a_sv);
      const f32x4 u4 = *(LAS f32x4*)(L + a_u) + *(LAS f32x4*)(L + a_u + 16);
      float sq = sv.x + sv.z, sm = sv.y + sv.w;
      sq = tr16(sq, sq); sm = tr16(sm, sm);
      sq = tr32(sq, sq); sm = tr32(sm, sm);
      const float us = (u4.x + u4.y) + (u4.z + u4.w);
      const float mean = sm * (1.f / DM);
      const float var = fmaxf(fmaf(sq, 1.f / DM, -mean * mean), 0.f);
      const float rd = __builtin_amdgcn_rcpf(rbf(__builtin_amdgcn_sqrtf(rbf(rbf(var) + 1e-5f))));
      const float z = rbf(rbf((us - rbf(mean) * cs) * rd) + bias);
      constexpr float GC = -2.f * LOG2E * 0.7978845608028654f;
      const float g = z * __builtin_amdgcn_rcpf(1.f + __builtin_amdgcn_exp2f(z * fmaf(z * z, GC * 0.044715f, GC)));
      const unsigned g0 = pk2(dpp<0x44>(g), dpp<0xEE>(g));
      const v4u gb = {g0, dppu<ROR12>(g0), dppu<ROR8>(g0), dppu<ROR4>(g0)};
      const f32x4 e0 = mfma(bf[4], gb, z4), e1 = mfma(bf[5], gb, z4), e2 = mfma(bf[6], gb, z4), e3 = mfma(bf[7], gb, z4);
      const f32x4 d = (c & 8) ? ((c & 4) ? e3 : e2) : ((c & 4) ? e1 : e0);
      const v4u dv = {nosent(__float_as_uint(d.x)), nosent(__float_as_uint(d.y)), nosent(__float_as_uint(d.z)), nosent(__float_as_uint(d.w))};
      __builtin_amdgcn_raw_buffer_store_b128(dv, rs, a_pp, 0, AUX_SC1);
    } else if (is_m2 && w < 4) {
      __builtin_amdgcn_s_sleep(D2);
      v2u hw;
      _Pragma("nounroll") for (unsigned spins = 0; spins < SPIN_CAP; ++spins) {
        asm volatile("" ::: "memory");
        hw = __builtin_amdgcn_raw_buffer_load_b64(rs, a_ho, 0, AUX_SC1);
        if (!any_lane(hw.x == SENT || hw.y == SENT)) break;
      }
      const unsigned hs = (r & 2) ? hw.y : hw.x;
      float hv = (r & 1) ? hi16(hs) : lo16(hs);
      hv += dpp<ROR2>(hv); hv += dpp<ROR4>(hv); hv += dpp<ROR8>(hv);
      hv = rbf(hv);
      pin(hv);
      __builtin_amdgcn_s_sleep(D3);
      v4u p0, p1;
      poll2(p0, p1, rs, a_g0, a_g1);
      const f32x4 sa = __builtin_bit_cast(f32x4, p0) + __builtin_bit_cast(f32x4, p1);
      float u = tr16(tr32(sa.x, sa.z), tr32(sa.y, sa.w));
      u += dpp<ROR2>(u); u += dpp<ROR4>(u); u += dpp<ROR8>(u);
      const float o = hv + rbf(rbf(u) + bias);
      __builtin_amdgcn_raw_buffer_store_b16((u16)pk2(o, o), mkrs(out, NB * DM * 2), a_pp, 0, 0);
    }
  } else {
    __builtin_amdgcn_s_setprio(3);
    v4u cpf;
    {
      unsigned long long pc0; unsigned back, len;
      asm volatile("s_getpc_b64 %0\n.Lfb_here:\n\ts_mov_b32 %1, .Lfb_here - fused_block\n\ts_mov_b32 %2, .Lfb_end - fused_block"
                   : "=s"(pc0), "=s"(back), "=s"(len));
      pc0 -= back;
      cpf = ld128<0>(mkrs((const void*)pc0, (int)len), cg * 1024 + l16, 0);
    }
    const rsrc_t rx = mkrs(x, NB * DM * 2);
    const v2u e = __builtin_amdgcn_raw_buffer_load_b64(rs, OFF_EPOCH, 0, 0);
    const v4u xs0 = ld128<0>(rx, lane * 32, 0), xs1 = ld128<0>(rx, lane * 32 + 16, 0);
    const unsigned bw = __builtin_amdgcn_raw_buffer_load_b16(is_m1 ? mkrs(b1 + 32 * mrole, 64) : mkrs(b2 + 16 * mrole, is_m2 ? 32 : 0), (lane & 31) * 2, 0, 0);
    unsigned xo = __builtin_amdgcn_raw_buffer_load_b32(rx, kr == 0 ? ((lane >> 5) * DM + gq * 64 + (lane & 31) * 2) * 2 : OOB, 0, 0);
    v4u wr[16];
    {
      const int qc = cg * DH + (kr & 1) * 64;
      const rsrc_t rq = mkrs(wq + (size_t)(128 * (kr >> 1)) * (NH * DH) + qc, 128 * NH * DH * 2 - qc * 2);
#pragma unroll
      for (int j = 0; j < 16; ++j) wr[j] = ld128<AUX_NT>(rq, (lane >> 3) * (NH * DH * 2) + (lane & 7) * 16, j * 8 * NH * DH * 2);
    }
    *(LAS unsigned*)(L + L_ML + c * 4) = SENT;
    *(LAS unsigned*)(L + L_Q + 264) = 0u;
    lds_barrier();
    __builtin_amdgcn_raw_buffer_store_b128(v4u{SENT, SENT, SENT, SENT}, rs,
        is_t && r < 2 ? OFF_AP + (gq >> 2) * 16384 + r * 8192 + (((gq & 3) * 2 + (c >> 3)) * 16 + kr * 2) * 64 + (c & 7) * 16 : OOB, 0, AUX_SC1);
    __builtin_amdgcn_sched_barrier(0);
    v4u xn0, xn1;
    {
      float sm = 0.f;
#pragma unroll
      for (int k = 0; k < 4; ++k) { sm = dot2(xs0[k], 0x3F803F80u, sm); sm = dot2(xs1[k], 0x3F803F80u, sm); }
      const float mean = half_sum(sm) * (1.f / DM);
      float sq = 0.f;
#pragma unroll
      for (int k = 0; k < 4; ++k) {
        const float d0 = lo16(xs0[k]) - mean, d1 = hi16(xs0[k]) - mean, d2 = lo16(xs1[k]) - mean, d3 = hi16(xs1[k]) - mean;
        sq = fmaf(d0, d0, sq); sq = fmaf(d1, d1, sq); sq = fmaf(d2, d2, sq); sq = fmaf(d3, d3, sq);
      }
      const float var = half_sum(sq) * (1.f / DM);
      const float mm = rbf(mean), rr = 1.f / rbf(__builtin_amdgcn_sqrtf(rbf(rbf(var) + 1e-5f)));
#pragma unroll
      for (int k = 0; k < 4; ++k) {
        xn0[k] = pk2(rbf(lo16(xs0[k]) - mm) * rr, rbf(hi16(xs0[k]) - mm) * rr);
        xn1[k] = pk2(rbf(lo16(xs1[k]) - mm) * rr, rbf(hi16(xs1[k]) - mm) * rr);
      }
      *(LAS v4u*)(L + L_XN + lane * 32) = xn0;
      *(LAS v4u*)(L + L_XN + lane * 32 + 16) = xn1;
    }
    const unsigned e0 = __builtin_amdgcn_readfirstlane(e.x), e1 = __builtin_amdgcn_readfirstlane(e.y);
    const unsigned tagA = e0 + 1u, tagB = (e1 ^ (e0 * 0x9E3779B1u)) + 0x7F4A7C15u;
    *(LAS float*)(L + L_Q + 272 + (lane & 31) * 4) = lo16(bw);
    asm volatile("" : "+v"(xo) : "v"(cpf));
    {
#pragma unroll
      for (int j = 0; j < 16; ++j) *(LAS v4u*)(L + L_WQ + (8 * j + (lane >> 3)) * 144 + (lane & 7) * 16) = wr[j];
      const int ra = L_WQ + (8 * r + (c >> 2)) * 144 + (c & 3) * 8;
      f32x4 dq0 = z4, dq1 = z4, dq2 = z4, dq3 = z4;
#pragma unroll
      for (int t = 0; t < 4; ++t) {
        const v4u xb = *(LAS v4u*)(L + L_XN + (c >> 3) * 1024 + (kr >> 1) * 256 + t * 64 + r * 16);
        dq0 = mfma(trfrag(L + ra + t * 4608, 576), xb, dq0);
        dq1 = mfma(trfrag(L + ra + t * 4608 + 32, 576), xb, dq1);
        dq2 = mfma(trfrag(L + ra + t * 4608 + 64, 576), xb, dq2);
        dq3 = mfma(trfrag(L + ra + t * 4608 + 96, 576), xb, dq3);
      }
      const f32x4 d = (c & 4) ? ((c & 2) ? dq3 : dq2) : ((c & 2) ? dq1 : dq0);
      __builtin_amdgcn_raw_buffer_store_b128(v4u{__float_as_uint((c & 1) ? d.z : d.x), tagA, __float_as_uint((c & 1) ? d.w : d.y), tagB}, rs,
          OFF_QX + (cg >> 1) * 16384 + (c >> 3) * 8192 + (cg & 1) * 4096 + kr * 512 + (r * 8 + (c & 7)) * 16, 0, AUX_SC1);
    }
    int near = 0;
    {
      const int qx_off = OFF_QX + (h >> 1) * 16384 + b * 8192 + (h & 1) * 4096 + l16;
      v4u qx[4];
      pace(t0 + QWAIT);
#pragma unroll
      for (int k = 0; k < 4; ++k) qx[k] = ld128<AUX_SC1>(rs, qx_off, k * 1024);
      __builtin_amdgcn_sched_barrier(0);
      {
        const int alt = (cg >> 2) == kr;
        const unsigned char* pp = ws + OFF_QX + ((cg >> 1) ^ (2 * alt)) * 16384 + (cg & 1) * 4096 + (kr ^ alt) * 512;
        unsigned m0 = ~0u, m1 = ~0u;
        _Pragma("nounroll") for (int i = 0; i < NPROBE; ++i) {
          unsigned long long ta, tb_, tc; unsigned d0, d1;
          asm volatile("s_memtime %0\n\ts_waitcnt lgkmcnt(0)\n\ts_load_dword %3, %5, 0\n\ts_waitcnt lgkmcnt(0)\n\t"
                       "s_memtime %1\n\ts_waitcnt lgkmcnt(0)\n\ts_load_dword %4, %5, 0x2000\n\ts_waitcnt lgkmcnt(0)\n\t"
                       "s_memtime %2\n\ts_waitcnt lgkmcnt(0)"
                       : "=&s"(ta), "=&s"(tb_), "=&s"(tc), "=&s"(d0), "=&s"(d1) : "s"(pp + i * 128) : "memory");
          m0 = min(m0, (unsigned)tb_ - (unsigned)ta); m1 = min(m1, (unsigned)tc - (unsigned)tb_);
        }
        near = m1 < m0 ? 8192 : 0;
      }
      _Pragma("nounroll") for (unsigned spins = 0; spins < SPIN_CAP; ++spins) {
        bool bad = false;
#pragma unroll
        for (int k = 0; k < 4; ++k) bad |= (qx[k].y != tagA) | (qx[k].w != tagB);
        if (!any_lane(bad)) break;
        __builtin_amdgcn_s_sleep(8);
        asm volatile("" ::: "memory");
#pragma unroll
        for (int k = 0; k < 4; ++k) qx[k] = ld128<AUX_SC1>(rs, qx_off, k * 1024);
      }
      float q0 = 0.f, q1 = 0.f;
#pragma unroll
      for (int k = 0; k < 4; ++k) { q0 += __uint_as_float(qx[k].x); q1 += __uint_as_float(qx[k].z); }
      *(LAS unsigned*)(L + L_Q + ((lane >> 5) * 64 + ((lane >> 1) & 3) * 16 + ((lane >> 3) & 3) * 4 + (lane & 1) * 2) * 2) = pk2(q0, q1);
      asm volatile("s_waitcnt lgkmcnt(0)" ::: "memory");
      *(volatile LAS unsigned*)(L + L_Q + 264) = 1u;
    }
    *(LAS int*)(L + L_Q + 268) = near;
    {
      __builtin_amdgcn_raw_buffer_store_b128(v4u{SENT, SENT, SENT, SENT}, rs,
          lane < 16 ? OFF_OP + kr * 16384 + near + ((cg & 3) * 8 + b * 4 + s) * 256 + c * 16 : OOB, 0, AUX_SC1);
      const rsrc_t rpo = mkrs(ws + OFF_PO + mrole * 16384 + near, is_m2 ? 4 * PO_CG : 0);
#pragma unroll
      for (int k = 0; k < 6; ++k) __builtin_amdgcn_raw_buffer_store_b128(v4u{SENT, SENT, SENT, SENT}, rpo, l16 + k * 1024, 0, AUX_SC1);
    }
    v2u ml;
    _Pragma("nounroll") for (unsigned spins = 0; spins < (SPIN_CAP << 4); ++spins) {
      const unsigned long long t = *(volatile LAS unsigned long long*)(L + L_ML + (lane & 7) * 8);
      ml = v2u{(unsigned)t, (unsigned)(t >> 32)};
      if (!any_lane(ml.y == SENT)) break;
      __builtin_amdgcn_s_sleep(4);
    }
    const float mwv = __uint_as_float(ml.x);
    float mg = fmaxf(mwv, dpp<XOR1>(mwv)); mg = fmaxf(mg, dpp<XOR2>(mg)); mg = fmaxf(mg, dpp<XOR7>(mg));
    float lg = __uint_as_float(ml.y) * __builtin_amdgcn_exp2f((mwv - mg) * LOG2E);
    lg += dpp<XOR1>(lg); lg += dpp<XOR2>(lg); lg += dpp<XOR7>(lg);
    asm volatile("s_waitcnt vmcnt(0)" ::: "memory");
    __builtin_amdgcn_raw_buffer_store_b128(v4u{__float_as_uint(mg), tagA, __float_as_uint(lg), tagB + (near >> 13)}, rs,
                                           lane == 0 ? OFF_FX + (kr * 32 + cg) * 16 : OOB, 0, AUX_SC1);
    float alpha;
    {
      const bool all = is_m1 || is_m2;
      const int f0 = OFF_FX + kr * 512 + (is_t ? lane & 31 : (b * 4 + (lane & 3)) * 4 + (cg & 3)) * 16;
      const rsrc_t rfa = mkrs(ws + OFF_FX, all ? 4096 : 0);
      v4u fs;
      pace(t0 + FWAIT + cg * FST);
      _Pragma("nounroll") for (unsigned spins = 0; spins < SPIN_CAP; ++spins) {
        asm volatile("" ::: "memory");
        fs = ld128<AUX_SC1>(rs, f0, 0);
        v4u fa[4];
#pragma unroll
        for (int k = 0; k < 4; ++k) fa[k] = ld128<AUX_SC1>(rfa, l16, k * 1024);
        bool bad = fs.y != tagA || fs.w - tagB > 1u;
        if (all) {
#pragma unroll
          for (int k = 0; k < 4; ++k) {
            bad |= fa[k].y != tagA || fa[k].w - tagB > 1u;
            *(LAS unsigned*)(L + L_NT + (k * 64 + lane) * 4) = (fa[k].w - tagB) & 1u;
          }
        } else *(LAS unsigned*)(L + L_NT + lane * 4) = (fs.w - tagB) & 1u;
        if (!any_lane(bad)) break;
        __builtin_amdgcn_s_sleep(24);
      }
      __builtin_amdgcn_raw_buffer_store_b32(tagA, rs, (bid == M2_BASE && lane == 0) ? OFF_EPOCH : OOB, 0, AUX_SC1);
      const bool sib = !is_t || ((lane & 3) == (cg & 3) && ((lane >> 4) & 1) == b);
      const float m_i = sib ? __uint_as_float(fs.x) : -INFINITY;
      const float M = wave_max(m_i);
      const float Ls = wave_sum(sib ? __uint_as_float(fs.z) * __builtin_amdgcn_exp2f((m_i - M) * LOG2E) : 0.f) * (is_t ? 0.5f : 0.0625f);
      alpha = __builtin_amdgcn_exp2f((mwv - M) * LOG2E) / Ls;
    }
    float al[8];
#pragma unroll
    for (int ww = 0; ww < 8; ++ww) al[ww] = __uint_as_float(__builtin_amdgcn_readlane(__float_as_uint(alpha), ww));
    int ap_st = is_t && (lane & 2) == 0 ? OFF_AP + (gq >> 2) * 16384 + (lane & 1) * 8192 + (((gq & 3) * 2 + ((lane >> 4) & 1)) * 16 + kr * 2 + (lane >> 5)) * 64 + ((lane >> 2) & 3) * 16 : OOB;
    int op_st = (lane & 3) == 0 ? OFF_OP + kr * 16384 + near + ((cg & 3) * 8 + b * 4 + s) * 256 + lane * 4 : OOB;
    int a_o = L_OP + lane * 8, a_p = L_P1 + lane * 8;
    asm volatile("" : "+v"(ap_st), "+v"(op_st), "+v"(a_o), "+v"(a_p));
    if (is_t) __builtin_amdgcn_s_barrier();
    lds_barrier();
    {
      f32x2 o[8];
#pragma unroll
      for (int ww = 0; ww < 8; ++ww) o[ww] = *(LAS f32x2*)(L + a_o + ww * DH * 4);
      const f32x2 o2 = ((o[0] * al[0] + o[1] * al[1]) + (o[2] * al[2] + o[3] * al[3])) + ((o[4] * al[4] + o[5] * al[5]) + (o[6] * al[6] + o[7] * al[7]));
      const unsigned pw = nosent(pk2(o2.x, o2.y));
      __builtin_amdgcn_raw_buffer_store_b128(v4u{dppu<0x00>(pw), dppu<0x55>(pw), dppu<0xAA>(pw), dppu<0xFF>(pw)}, rs, op_st, 0, AUX_SC1);
    }
    if (is_t || is_m1) lds_barrier();
    if (is_t) {
      f32x2 p[8];
#pragma unroll
      for (int ww = 0; ww < 8; ++ww) p[ww] = *(LAS f32x2*)(L + a_p + ww * 512);
      const f32x2 u = (((p[0] + p[1]) + (p[2] + p[3])) + ((p[4] + p[5]) + (p[6] + p[7]))) + f32x2{lo16(xo), hi16(xo)};
      const unsigned pw = nosent(pk2(u.x, u.y));
      __builtin_amdgcn_raw_buffer_store_b128(v4u{dppu<0x00>(pw), dppu<0x55>(pw), dppu<0xAA>(pw), dppu<0xFF>(pw)}, rs, ap_st, 0, AUX_SC1);
    }
    asm volatile(".Lfb_end:");
  }
}

extern "C" void kernel_launch(void* const* d_in, const int* in_sizes, int n_in,
                              void* d_out, int out_size, void* d_ws, size_t ws_size,
                              hipStream_t stream) {
  fused_block<<<NWG, NTHR, 0, stream>>>((const u16*)d_in[0], (const u16*)d_in[1], (const u16*)d_in[2],
                                        (const u16*)d_in[3], (const u16*)d_in[4], (const u16*)d_in[6],
                                        (const u16*)d_in[7], (const u16*)d_in[8], (const u16*)d_in[9],
                                        (u16*)d_out, (unsigned char*)d_ws);
}
```
